```python
import torch

def _softmax(x, axis=-1):
    return torch.softmax(x, dim=axis)
def _gelu(x):
    return torch.nn.functional.gelu(x, approximate="tanh")
def _silu(x):
    return torch.nn.functional.silu(x)
def _sigmoid(x):
    return torch.sigmoid(x)
def _layernorm(x, axis=-1, eps=1e-5):
    m = x.mean(dim=axis, keepdim=True)
    v = x.var(dim=axis, keepdim=True, unbiased=False)
    return (x - m) / torch.sqrt(v + eps)
def _scatter_add(tgt, idx, src, axis=0):
    return tgt.clone().index_add_(axis, idx, src)
def _segment_sum(src, seg, n):
    out = torch.zeros((n,) + src.shape[1:], dtype=src.dtype)
    return out.index_add_(0, seg, src)
def _pad(x, pw):
    flat = tuple(v for pair in reversed(pw) for v in pair)
    return torch.nn.functional.pad(x, flat)

kernel_name = 'interleave_layers__spectral_gfnet__mixer_channel_mlp__prompt'

def setup_inputs():

    _w = (lambda a: (lambda t: t.to(torch.bfloat16) if t.is_floating_point() else t)(torch.as_tensor(a)))
    def _A_setup():
        B, S, D = 1, 2048, 448
        Df = D // 2 + 1
        return {
            "x": _w((lambda *s: torch.randn(*s, dtype=torch.bfloat16) * 0.05)(B, S, D)),
            "w_r": _w((lambda *s: torch.randn(*s, dtype=torch.bfloat16) * 0.8)(S, Df) * 0.02),
            "w_i": _w((lambda *s: torch.randn(*s, dtype=torch.bfloat16) * 0.8)(S, Df) * 0.02),
        }
    _A_in = _A_setup()

    def _B_setup():
        B, S, D, F = 1, 2048, 448, 1792
        return {
            "x": _w((lambda *s: torch.randn(*s, dtype=torch.bfloat16) * 0.05)(B, S, D)),
            "w1": _w((lambda *s: torch.randn(*s, dtype=torch.bfloat16) * 0.8)(D, F) * (D ** -0.5)),
            "b1": _w((lambda *s: torch.randn(*s, dtype=torch.bfloat16) * 0.4)(F)),
            "w2": _w((lambda *s: torch.randn(*s, dtype=torch.bfloat16) * 0.05)(F, D) * (F ** -0.5)),
            "b2": _w((lambda *s: torch.randn(*s, dtype=torch.bfloat16) * 0.05)(D)),
        }
    _B_in = _B_setup()

    return {
        "A_x": _A_in["x"],
        "A_w_r": _A_in["w_r"],
        "A_w_i": _A_in["w_i"],
        "B_x": _B_in["x"],
        "B_w1": _B_in["w1"],
        "B_b1": _B_in["b1"],
        "B_w2": _B_in["w2"],
        "B_b2": _B_in["b2"],
    }

def reference(A_x, A_w_r, A_w_i, B_x, B_w1, B_b1, B_w2, B_b2):
    def _A_ref(x, w_r, w_i):
        B, S, D = x.shape
        X = (lambda x, axes=(-2,-1): torch.fft.rfft2(x.float(), dim=axes))(x, axes=(-2, -1))
        W = w_r + 1j * w_i
        X = X * W
        y = (lambda x, s, axes=(-2,-1): torch.fft.irfft2(x, s=s, dim=axes).to(torch.bfloat16))(X, (S, D), axes=(-2, -1))
        return y

    def _B_ref(x, w1, b1, w2, b2):
        y = _layernorm(x, axis=-1)
        h = _gelu(torch.einsum("bsd,df->bsf", y, w1) + b1)
        return torch.einsum("bsf,fd->bsd", h, w2) + b2

    x = A_x
    x = x + _A_ref(_layernorm(x, axis=-1), A_w_r, A_w_i)
    x = x + _B_ref(_layernorm(x, axis=-1), B_w1, B_b1, B_w2, B_b2)
    x = x + _A_ref(_layernorm(x, axis=-1), A_w_r, A_w_i)
    x = x + _B_ref(_layernorm(x, axis=-1), B_w1, B_b1, B_w2, B_b2)
    return x
```

```cpp
#include <hip/hip_runtime.h>
#include <hip/hip_bf16.h>
#include <stdint.h>

typedef __bf16 bf16x8 __attribute__((ext_vector_type(8)));
typedef __bf16 bf16x2 __attribute__((ext_vector_type(2)));
typedef short s16x4 __attribute__((ext_vector_type(4)));
typedef float f32x2 __attribute__((ext_vector_type(2)));
typedef float f32x4 __attribute__((ext_vector_type(4)));
typedef float f32x16 __attribute__((ext_vector_type(16)));
typedef unsigned u32x2 __attribute__((ext_vector_type(2)));
typedef unsigned u32x4 __attribute__((ext_vector_type(4)));
typedef unsigned char u8;
typedef unsigned short u16;
typedef __amdgpu_buffer_rsrc_t rsrc_t;
#define LAS __attribute__((address_space(3)))
#define DEV __device__ __forceinline__

constexpr unsigned XT_B = 448u * 2048u * 2u;
constexpr unsigned FR_B = 1605632u;
constexpr unsigned H_B = 7340032u;
constexpr unsigned O_CTRL = 0;
constexpr unsigned O_XT0 = 65536;
constexpr unsigned O_XT1 = O_XT0 + XT_B;
constexpr unsigned O_XT2 = O_XT1 + XT_B;
constexpr unsigned O_XT3 = O_XT2 + XT_B;
constexpr unsigned O_R0 = O_XT3 + XT_B;
constexpr unsigned O_SS = O_R0 + 8192;
constexpr unsigned O_WT = O_SS + 57344;
constexpr unsigned O_W1F = O_WT + XT_B;
constexpr unsigned O_W2F = O_W1F + FR_B;
constexpr unsigned O_W2O = O_W2F + FR_B;
constexpr unsigned O_QT = O_W2O + FR_B;
constexpr unsigned O_B2H = O_QT + 401408;
constexpr unsigned O_H1 = O_B2H + 2048;
constexpr unsigned O_H2 = O_H1 + H_B;
constexpr unsigned WS_END = O_H2 + H_B;

constexpr float EPS = 1e-5f;
constexpr float SC1 = 0.04724555912615340f;
constexpr float SC2 = 0.06681531047810609f;

DEV unsigned pack2(float a, float b) {
	f32x2 v = {a, b};
	return __builtin_bit_cast(unsigned, __builtin_convertvector(v, bf16x2));
}
DEV float bflo(unsigned u) { return __builtin_bit_cast(float, u << 16); }
DEV float bfhi(unsigned u) { return __builtin_bit_cast(float, u & 0xffff0000u); }
DEV f32x4 mfma16(u32x4 a, u32x4 b, f32x4 c) {
	return __builtin_amdgcn_mfma_f32_16x16x32_bf16(__builtin_bit_cast(bf16x8, a), __builtin_bit_cast(bf16x8, b), c, 0, 0, 0);
}
DEV f32x16 mfma32(u32x4 a, u32x4 b, f32x16 c) {
	return __builtin_amdgcn_mfma_f32_32x32x16_bf16(__builtin_bit_cast(bf16x8, a), __builtin_bit_cast(bf16x8, b), c, 0, 0, 0);
}
DEV rsrc_t mkrs(const void* p, unsigned bytes) { return __builtin_amdgcn_make_buffer_rsrc((void*)p, 0, bytes, 0x00020000); }
DEV void dma16(rsrc_t rs, u8* lds, unsigned voff, unsigned soff) {
	__builtin_amdgcn_raw_ptr_buffer_load_lds(rs, (LAS void*)lds, 16, voff, soff, 0, 0);
}
DEV u32x2 trread(const u8* lds) {
	return __builtin_bit_cast(u32x2, __builtin_amdgcn_ds_read_tr16_b64_v4i16((LAS s16x4*)lds));
}
DEV u32x4 cat(u32x2 a, u32x2 b) { return u32x4{a[0], a[1], b[0], b[1]}; }
DEV u32x4 ldg16(const u8* p) { return *(const u32x4*)p; }
DEV float cosr(float x) { return __builtin_amdgcn_cosf(x); }
DEV float sinr(float x) { return __builtin_amdgcn_sinf(x); }
#define WAIT_BAR(N) asm volatile("s_waitcnt vmcnt(" #N ") lgkmcnt(0)\n\ts_barrier" ::: "memory")

DEV void basis(int j, float& kq, float& ph) {
	int c = j >> 1;
	int kk = c ? c : ((j & 1) ? 0 : 224);
	kq = (float)kk * (1.0f / 448.0f);
	ph = (c && (j & 1)) ? 0.25f : 0.0f;
}

DEV void p0_rows_dma(rsrc_t rs, u8* sm, int w, int lane, unsigned row0, unsigned rstride) {
	if (lane < 56) {
#pragma unroll
		for (int n = 0; n < 16; ++n) {
			int r = 16 * w + n;
			unsigned sg = 2 * w + ((n >> 1) & 1);
			dma16(rs, sm + r * 896, ((unsigned)lane ^ sg) * 16, row0 + r * rstride);
		}
	}
}
DEV void p0_rows_gemm(const u8* sm, const u32x4 (&qf)[14], f32x4 (&acc)[4], int g, int i16) {
	unsigned sg = 2 * (i16 >> 2) + ((i16 >> 1) & 1);
	const u8* base = sm + (16 * (i16 >> 2) + (i16 & 3)) * 896;
	const u8* b0 = base + ((g ^ sg) * 16);
	const u8* b1 = base + (((4 + g) ^ sg) * 16);
#pragma unroll
	for (int t = 0; t < 4; ++t) acc[t] = f32x4{0.f, 0.f, 0.f, 0.f};
#pragma unroll
	for (int ks = 0; ks < 14; ++ks) {
#pragma unroll
		for (int t = 0; t < 4; ++t) {
			u32x4 a = *(const u32x4*)(((ks & 1) ? b1 : b0) + (ks >> 1) * 128 + t * 3584);
			acc[t] = mfma16(a, qf[ks], acc[t]);
		}
	}
}

__global__ __launch_bounds__(256, 1) void k_p0(const u8* __restrict__ x, const u16* __restrict__ wr, const u16* __restrict__ wi,
		const u8* __restrict__ w1, const u8* __restrict__ w2, const u16* __restrict__ b2, u8* __restrict__ ws) {
	__shared__ __attribute__((aligned(16))) u8 sm[114688];
	const int tid = threadIdx.x, lane = tid & 63, w = __builtin_amdgcn_readfirstlane(tid >> 6);
	const int g = lane >> 4, i16 = lane & 15;
	const int b = blockIdx.x;
	if (b < 224) {
		const int slot = b >> 3, pt = 4 * (b & 7) + slot / 7, jt = slot % 7;
		const bool fold = pt < 28;
		u8* bufX = sm;
		u8* bufW = sm + 57344;
		p0_rows_dma(mkrs(x, 2048u * 896u), bufX, w, lane, pt * 896u, 32u * 896u);
		if (fold) {
			rsrc_t rs = mkrs(w1, 448u * 3584u);
			unsigned v0 = (lane >> 3) * 3584 + (((lane & 7) ^ (((lane >> 4) & 1) << 1)) * 16);
			unsigned v1 = (lane >> 3) * 3584 + (((lane & 7) ^ ((((lane >> 4) & 1) | 2) << 1)) * 16);
#pragma unroll
			for (int n = 0; n < 14; ++n) {
				int i = 14 * w + n;
				dma16(rs, bufW + i * 1024, (n & 1) ? v1 : v0, i * (8 * 3584) + pt * 128);
			}
		}
		const int j = 64 * jt + 16 * w + i16;
		float kq, ph;
		basis(j, kq, ph);
		const float a0 = (float)(8 * g) * kq + ph;
		u32x4 qf[14];
#pragma unroll
		for (int ks = 0; ks < 14; ++ks) {
#pragma unroll
			for (int i = 0; i < 4; ++i)
				qf[ks][i] = pack2(cosr(__builtin_fmaf(kq, (float)(32 * ks + 2 * i), a0)), cosr(__builtin_fmaf(kq, (float)(32 * ks + 2 * i + 1), a0)));
		}
		const float scj = (j < 2) ? SC1 : SC2;
		if (fold) WAIT_BAR(14); else WAIT_BAR(0);
		f32x4 acc[4];
		p0_rows_gemm(bufX, qf, acc, g, i16);
		if (jt < 4) {
			const int r = 16 * jt + 4 * w + g;
			const u8* rb = bufX + r * 896 + i16 * 16;
			float s1 = 0.f, s2 = 0.f;
#pragma unroll
			for (int q = 0; q < 4; ++q) {
				u32x4 v = {0u, 0u, 0u, 0u};
				if (q < 3 || i16 < 8) v = *(const u32x4*)(rb + q * 256);
#pragma unroll
				for (int i = 0; i < 4; ++i) {
					float lo = bflo(v[i]), hi = bfhi(v[i]);
					s1 += lo + hi;
					s2 = __builtin_fmaf(lo, lo, __builtin_fmaf(hi, hi, s2));
				}
			}
#pragma unroll
			for (int m = 1; m < 16; m <<= 1) {
				s1 += __shfl_xor(s1, m);
				s2 += __shfl_xor(s2, m);
			}
			float mean = s1 * (1.0f / 448.0f);
			float var = fmaxf(s2 * (1.0f / 448.0f) - mean * mean, 0.f);
			if (i16 == 0) ((float*)(ws + O_R0))[64 * pt + r] = __builtin_amdgcn_rsqf(var + EPS);
		}
		WAIT_BAR(0);
		if (fold) p0_rows_dma(mkrs(w2, 1792u * 896u), bufX, w, lane, pt * (64u * 896u), 896u);
		{
			u8* o = ws + O_XT0 + j * 4096 + pt * 128 + g * 32;
			u32x4 o0 = {pack2(acc[0][0] * scj, acc[0][1] * scj), pack2(acc[0][2] * scj, acc[0][3] * scj),
						pack2(acc[1][0] * scj, acc[1][1] * scj), pack2(acc[1][2] * scj, acc[1][3] * scj)};
			u32x4 o1 = {pack2(acc[2][0] * scj, acc[2][1] * scj), pack2(acc[2][2] * scj, acc[2][3] * scj),
						pack2(acc[3][0] * scj, acc[3][1] * scj), pack2(acc[3][2] * scj, acc[3][3] * scj)};
			*(u32x4*)o = o0;
			*(u32x4*)(o + 16) = o1;
		}
		if (!fold) return;
		{
			const int q = (lane >> 2) & 3, pc = lane & 3;
			const unsigned kap = ((q >> 1) & 1) | ((g & 1) << 1);
			const u8* rb = bufW + (8 * g + q) * 128 + (pc & 1) * 8;
#pragma unroll
			for (int t = 0; t < 4; ++t) acc[t] = f32x4{0.f, 0.f, 0.f, 0.f};
#pragma unroll
			for (int ks = 0; ks < 14; ++ks) {
#pragma unroll
				for (int t = 0; t < 4; ++t) {
					const u8* p = rb + (((2 * t + (pc >> 1)) ^ (kap << 1)) * 16) + ks * 4096;
					acc[t] = mfma16(qf[ks], cat(trread(p), trread(p + 512)), acc[t]);
				}
			}
			const int j0 = 64 * jt + 16 * w + 4 * g;
			const float s0 = (j0 == 0) ? SC1 : SC2, s1 = (j0 == 0) ? 0.f : SC2;
#pragma unroll
			for (int u = 0; u < 2; ++u) {
				unsigned x0 = pack2(acc[2 * u][0] * s0, acc[2 * u][1] * s1), x1 = pack2(acc[2 * u][2] * SC2, acc[2 * u][3] * SC2);
				unsigned y0 = pack2(acc[2 * u + 1][0] * s0, acc[2 * u + 1][1] * s1), y1 = pack2(acc[2 * u + 1][2] * SC2, acc[2 * u + 1][3] * SC2);
				auto r0 = __builtin_amdgcn_permlane16_swap(x0, y0, false, false);
				auto r1 = __builtin_amdgcn_permlane16_swap(x1, y1, false, false);
				*(u32x4*)(ws + O_W1F + ((2 * pt + u) * 28 + 4 * jt + w) * 1024 + lane * 16) = u32x4{r0[0], r1[0], r0[1], r1[1]};
			}
		}
		WAIT_BAR(0);
		p0_rows_gemm(bufX, qf, acc, g, i16);
#pragma unroll
		for (int h = 0; h < 2; ++h) {
			u32x4 o = {pack2(acc[h][0] * scj, acc[h][1] * scj), pack2(acc[h][2] * scj, acc[h][3] * scj),
					   pack2(acc[2 + h][0] * scj, acc[2 + h][1] * scj), pack2(acc[2 + h][2] * scj, acc[2 + h][3] * scj)};
			*(u32x4*)(ws + O_W2F + ((2 * jt + (w >> 1)) * 112 + 4 * pt + g) * 1024 + (32 * h + 16 * (w & 1) + i16) * 16) = o;
		}
		{
			const int q = (lane >> 2) & 3, pc = lane & 3, h = lane >> 5;
			const int r0 = 16 * w + 4 * h + q;
			const unsigned sg = 2 * w + ((q >> 1) & 1);
#pragma unroll
			for (int u = 0; u < 2; ++u) {
				unsigned ch = 8 * jt + ((4 * u + 2 * (g & 1) + (pc >> 1)) ^ sg);
				const u8* p = bufX + r0 * 896 + ch * 16 + (pc & 1) * 8;
				*(u32x4*)(ws + O_W2O + ((2 * jt + u) * 112 + 4 * pt + w) * 1024 + lane * 16) = cat(trread(p), trread(p + 8 * 896));
			}
		}
	} else {
		const int sb = b - 224;
		if (lane < 56) {
#pragma unroll 2
			for (int n = 0; n < 8; ++n) {
				int job = sb * 8 + n, f1 = job & 63, k = 1 + 56 * (job >> 6) + lane;
				unsigned v[8];
#pragma unroll
				for (int i = 0; i < 8; ++i) {
					int f = f1 + 64 * (16 * (i >> 2) + 4 * w + (i & 3));
					v[i] = (unsigned)wr[f * 225 + k] | ((unsigned)wi[f * 225 + k] << 16);
				}
				int c = (k == 224) ? 0 : k;
				u8* o = ws + O_WT + c * 8192 + f1 * 128 + w * 32;
				*(u32x4*)o = u32x4{v[0], v[1], v[2], v[3]};
				*(u32x4*)(o + 16) = u32x4{v[4], v[5], v[6], v[7]};
			}
		}
		for (int fr = sb * 4 + w; fr < 392; fr += 128) {
			int dt = fr / 28, ks = fr % 28;
			float d = (float)(32 * dt + (lane & 31));
			float v[8];
#pragma unroll
			for (int i = 0; i < 8; ++i) {
				int j = 16 * ks + 8 * (lane >> 5) + i;
				float kq, ph;
				basis(j, kq, ph);
				v[i] = cosr(__builtin_fmaf(kq, d, ph)) * ((j < 2) ? SC1 : SC2);
			}
			*(u32x4*)(ws + O_QT + fr * 1024 + lane * 16) = u32x4{pack2(v[0], v[1]), pack2(v[2], v[3]), pack2(v[4], v[5]), pack2(v[6], v[7])};
		}
		for (int jj = w; jj < 14; jj += 4) {
			int j = sb * 14 + jj;
			float kq, ph, s = 0.f;
			basis(j, kq, ph);
#pragma unroll
			for (int i = 0; i < 7; ++i) {
				int d = lane + 64 * i;
				s = __builtin_fmaf(bflo(b2[d]), cosr(__builtin_fmaf(kq, (float)d, ph)), s);
			}
#pragma unroll
			for (int m = 1; m < 64; m <<= 1) s += __shfl_xor(s, m);
			if (lane == 0) ((float*)(ws + O_B2H))[j] = s * ((j < 2) ? SC1 : SC2);
		}
	}
}

template <bool FIRST>
__global__ __launch_bounds__(256, 1) void k_col(u8* __restrict__ ws) {
	__shared__ __attribute__((aligned(16))) u8 sm[20480];
	const int tid = threadIdx.x, lane = tid & 63, w = __builtin_amdgcn_readfirstlane(tid >> 6);
	const int g = lane >> 4, i16 = lane & 15;
	const int c = blockIdx.x;
	const u8* xin = ws + (FIRST ? O_XT0 : O_XT2) + c * 8192;
	u8* xout = ws + (FIRST ? O_XT1 : O_XT3) + c * 8192;
	const int p0 = 64 * i16 + 16 * w + 4 * g;
	u32x2 xr[2], xi[2];
	f32x4 rr[2];
#pragma unroll
	for (int t = 0; t < 2; ++t) {
		xr[t] = *(const u32x2*)(xin + (p0 + 1024 * t) * 2);
		xi[t] = *(const u32x2*)(xin + 4096 + (p0 + 1024 * t) * 2);
		if (FIRST) rr[t] = *(const f32x4*)(ws + O_R0 + (p0 + 1024 * t) * 4);
		else {
			f32x4 s = *(const f32x4*)(ws + O_SS + (p0 + 1024 * t) * 4);
#pragma unroll
			for (int k = 1; k < 7; ++k) s += *(const f32x4*)(ws + O_SS + k * 8192 + (p0 + 1024 * t) * 4);
#pragma unroll
			for (int e = 0; e < 4; ++e) rr[t][e] = __builtin_amdgcn_rsqf(__builtin_fmaf(s[e], 1.0f / 448.0f, EPS));
		}
	}
	u32x4 wv[2];
	wv[0] = ldg16(ws + O_WT + c * 8192 + (16 * w + i16) * 128 + g * 32);
	wv[1] = ldg16(ws + O_WT + c * 8192 + (16 * w + i16) * 128 + g * 32 + 16);
	u32x4 C1[2], S1[2], N1[2], C2[2], S2[2], N2[2];
	float twc[8], tws[8], tic[8], tis[8];
	{
		const float f1 = (float)(16 * w + i16);
#pragma unroll
		for (int ks = 0; ks < 2; ++ks)
#pragma unroll
			for (int i = 0; i < 4; ++i) {
				float a = f1 * (float)(32 * ks + 8 * g + 2 * i) * (1.0f / 64.0f), bb = a + f1 * (1.0f / 64.0f);
				C1[ks][i] = pack2(cosr(a), cosr(bb));
				S1[ks][i] = pack2(sinr(a), sinr(bb));
				N1[ks][i] = S1[ks][i] ^ 0x80008000u;
			}
#pragma unroll
		for (int t2 = 0; t2 < 2; ++t2)
#pragma unroll
			for (int i = 0; i < 4; ++i) {
				float m = (float)(16 * t2 + i16) * (1.0f / 32.0f);
				float a = m * (float)(16 * (i >> 1) + 4 * g + 2 * (i & 1)), bb = a + m;
				C2[t2][i] = pack2(cosr(a), cosr(bb));
				S2[t2][i] = pack2(sinr(a), sinr(bb));
				N2[t2][i] = S2[t2][i] ^ 0x80008000u;
			}
#pragma unroll
		for (int i = 0; i < 8; ++i) {
			float a = f1 * (float)(16 * (i >> 2) + 4 * g + (i & 3)) * (1.0f / 2048.0f);
			twc[i] = cosr(a);
			tws[i] = sinr(a);
			float bb = (float)(16 * w + 4 * g + (i & 3)) * (float)(16 * (i >> 2) + i16) * (1.0f / 2048.0f);
			tic[i] = cosr(bb) * (1.0f / 2048.0f);
			tis[i] = sinr(bb) * (1.0f / 2048.0f);
		}
	}
	const unsigned imask = c ? 0xffffffffu : 0u;
#pragma unroll
	for (int t = 0; t < 2; ++t) {
		u8* p = sm + (16 * t + i16) * 160 + 32 * w + 8 * g;
		*(u32x2*)p = u32x2{pack2(bflo(xr[t][0]) * rr[t][0], bfhi(xr[t][0]) * rr[t][1]), pack2(bflo(xr[t][1]) * rr[t][2], bfhi(xr[t][1]) * rr[t][3])};
		*(u32x2*)(p + 5120) = u32x2{pack2(bflo(xi[t][0]) * rr[t][0], bfhi(xi[t][0]) * rr[t][1]) & imask, pack2(bflo(xi[t][1]) * rr[t][2], bfhi(xi[t][1]) * rr[t][3]) & imask};
	}
	__syncthreads();
	const f32x4 z4 = {0.f, 0.f, 0.f, 0.f};
	f32x4 are[2], aim[2];
	u32x4 Bre, Bim;
#pragma unroll
	for (int t = 0; t < 2; ++t) {
		are[t] = z4; aim[t] = z4;
#pragma unroll
		for (int ks = 0; ks < 2; ++ks) {
			const u8* p = sm + (16 * t + i16) * 160 + 64 * ks + 16 * g;
			u32x4 mr = *(const u32x4*)p, mi = *(const u32x4*)(p + 5120);
			are[t] = mfma16(mr, C1[ks], are[t]);
			aim[t] = mfma16(mi, C1[ks], aim[t]);
			are[t] = mfma16(mi, S1[ks], are[t]);
			aim[t] = mfma16(mr, N1[ks], aim[t]);
		}
	}
#pragma unroll
	for (int t = 0; t < 2; ++t)
#pragma unroll
		for (int e = 0; e < 4; e += 2) {
			int i = 4 * t + e;
			Bre[i >> 1] = pack2(are[t][e] * twc[i] + aim[t][e] * tws[i], are[t][e + 1] * twc[i + 1] + aim[t][e + 1] * tws[i + 1]);
			Bim[i >> 1] = pack2(aim[t][e] * twc[i] - are[t][e] * tws[i], aim[t][e + 1] * twc[i + 1] - are[t][e + 1] * tws[i + 1]);
		}
#pragma unroll
	for (int t = 0; t < 2; ++t) {
		are[t] = mfma16(C2[t], Bre, z4);
		aim[t] = mfma16(C2[t], Bim, z4);
		are[t] = mfma16(S2[t], Bim, are[t]);
		aim[t] = mfma16(N2[t], Bre, aim[t]);
	}
#pragma unroll
	for (int t = 0; t < 2; ++t)
#pragma unroll
		for (int e = 0; e < 4; e += 2) {
			float a0 = bflo(wv[t][e]), b0 = bfhi(wv[t][e]), a1 = bflo(wv[t][e + 1]), b1 = bfhi(wv[t][e + 1]);
			Bre[2 * t + (e >> 1)] = pack2(are[t][e] * a0 - aim[t][e] * b0, are[t][e + 1] * a1 - aim[t][e + 1] * b1);
			Bim[2 * t + (e >> 1)] = pack2(are[t][e] * b0 + aim[t][e] * a0, are[t][e + 1] * b1 + aim[t][e + 1] * a1);
		}
#pragma unroll
	for (int t = 0; t < 2; ++t) {
		are[t] = mfma16(Bre, C2[t], z4);
		aim[t] = mfma16(Bim, C2[t], z4);
		are[t] = mfma16(Bim, N2[t], are[t]);
		aim[t] = mfma16(Bre, S2[t], aim[t]);
	}
#pragma unroll
	for (int t = 0; t < 2; ++t) {
		float vr[4], vi[4];
#pragma unroll
		for (int e = 0; e < 4; ++e) {
			vr[e] = are[t][e] * tic[4 * t + e] - aim[t][e] * tis[4 * t + e];
			vi[e] = aim[t][e] * tic[4 * t + e] + are[t][e] * tis[4 * t + e];
		}
		u8* p = sm + 10240 + (16 * t + i16) * 160 + 32 * w + 8 * g;
		*(u32x2*)p = u32x2{pack2(vr[0], vr[1]), pack2(vr[2], vr[3])};
		*(u32x2*)(p + 5120) = u32x2{pack2(vi[0], vi[1]), pack2(vi[2], vi[3])};
	}
	__syncthreads();
#pragma unroll
	for (int t = 0; t < 2; ++t) {
		are[t] = z4; aim[t] = z4;
#pragma unroll
		for (int ks = 0; ks < 2; ++ks) {
			const u8* p = sm + 10240 + (16 * t + i16) * 160 + 64 * ks + 16 * g;
			u32x4 mr = *(const u32x4*)p, mi = *(const u32x4*)(p + 5120);
			are[t] = mfma16(C1[ks], mr, are[t]);
			aim[t] = mfma16(C1[ks], mi, aim[t]);
			are[t] = mfma16(N1[ks], mi, are[t]);
			aim[t] = mfma16(S1[ks], mr, aim[t]);
		}
	}
	const float im = c ? 1.0f : 0.0f;
#pragma unroll
	for (int t = 0; t < 2; ++t) {
		*(u32x2*)(xout + (p0 + 1024 * t) * 2) = u32x2{pack2(bflo(xr[t][0]) + are[t][0], bfhi(xr[t][0]) + are[t][1]), pack2(bflo(xr[t][1]) + are[t][2], bfhi(xr[t][1]) + are[t][3])};
		*(u32x2*)(xout + 4096 + (p0 + 1024 * t) * 2) = u32x2{pack2(bflo(xi[t][0]) + im * aim[t][0], bfhi(xi[t][0]) + im * aim[t][1]), pack2(bflo(xi[t][1]) + im * aim[t][2], bfhi(xi[t][1]) + im * aim[t][3])};
	}
}

template <bool FIRST>
__global__ __launch_bounds__(256, 1) void k_g1(u8* __restrict__ ws, const u8* __restrict__ b1) {
	__shared__ __attribute__((aligned(16))) u8 sm[114688 + 512];
	const int tid = threadIdx.x, lane = tid & 63, w = __builtin_amdgcn_readfirstlane(tid >> 6);
	const int b = blockIdx.x, slot = b >> 3, pp = 2 * (b & 7) + slot / 14, ft = slot % 14;
	const int h = lane >> 5;
	u32x4 af[28];
	{
		const u8* ap = ws + O_W1F + (4 * ft + w) * (28 * 1024) + lane * 16;
#pragma unroll
		for (int ks = 0; ks < 28; ++ks) af[ks] = ldg16(ap + ks * 1024);
	}
	u32x2 bv[4];
#pragma unroll
	for (int q = 0; q < 4; ++q) bv[q] = *(const u32x2*)(b1 + (32 * (4 * ft + w) + 8 * q + 4 * h) * 2);
	{
		rsrc_t rs = mkrs(ws + (FIRST ? O_XT1 : O_XT3), XT_B);
		unsigned vo = (lane >> 4) * 4096 + (((lane & 15) ^ ((lane >> 4) << 2)) * 16);
#pragma unroll
		for (int n = 0; n < 28; ++n) dma16(rs, sm + (4 * n + w) * 1024, vo, (4 * n + w) * 16384 + pp * 256);
	}
	WAIT_BAR(0);
	f32x16 acc[4];
#pragma unroll
	for (int i = 0; i < 4; ++i)
#pragma unroll
		for (int r = 0; r < 16; ++r) acc[i][r] = 0.f;
	float ss = 0.f;
	{
		const int q = (lane >> 2) & 3, pc = lane & 3, cg = (lane >> 4) & 1;
		const u8* rb = sm + (8 * h + q) * 256 + (2 * cg + (pc >> 1)) * 16 + (pc & 1) * 8;
		const u8* tb[4];
#pragma unroll
		for (int i = 0; i < 4; ++i) tb[i] = rb + (((w + i) & 3) ^ q) * 64;
#pragma unroll
		for (int ks = 0; ks < 28; ++ks) {
#pragma unroll
			for (int i = 0; i < 4; ++i) {
				u32x4 bf = cat(trread(tb[i] + ks * 4096), trread(tb[i] + ks * 4096 + 1024));
				acc[i] = mfma32(af[ks], bf, acc[i]);
				if (i == 0) {
					if (ks == 0) bf[0] &= h ? 0xffffffffu : 0x0000ffffu;
#pragma unroll
					for (int e = 0; e < 4; ++e) {
						float lo = bflo(bf[e]), hi = bfhi(bf[e]);
						ss = __builtin_fmaf(lo, lo, __builtin_fmaf(hi, hi, ss));
					}
				}
			}
		}
	}
	ss += __shfl_xor(ss, 32);
	{
		float v = ss * (1.0f / 448.0f);
		float r1 = __builtin_amdgcn_rsqf(v + EPS);
		float r2 = r1 * __builtin_amdgcn_rsqf(__builtin_fmaf(v * r1, r1, EPS));
		if (lane < 32) ((float*)(sm + 114688))[32 * w + lane] = r2;
	}
	__syncthreads();
	u8* hp = ws + (FIRST ? O_H1 : O_H2) + (2 * (4 * ft + w)) * 1024 + lane * 16;
#pragma unroll
	for (int i = 0; i < 4; ++i) {
		const int tp = (w + i) & 3;
		const float r = ((const float*)(sm + 114688))[32 * tp + (lane & 31)];
		unsigned o[8];
#pragma unroll
		for (int k = 0; k < 8; ++k) {
			float y[2];
#pragma unroll
			for (int e = 0; e < 2; ++e) {
				float bb = e ? bfhi(bv[k >> 1][k & 1]) : bflo(bv[k >> 1][k & 1]);
				float pre = __builtin_fmaf(acc[i][2 * k + e], r, bb);
				float z = pre * __builtin_fmaf(pre * pre, -0.10294324f, -2.30220820f);
				y[e] = pre * __builtin_amdgcn_rcpf(1.0f + __builtin_amdgcn_exp2f(z));
			}
			o[k] = pack2(y[0], y[1]);
		}
		u8* p = hp + (4 * pp + tp) * (112 * 1024);
		*(u32x4*)p = u32x4{o[0], o[1], o[2], o[3]};
		*(u32x4*)(p + 1024) = u32x4{o[4], o[5], o[6], o[7]};
	}
}

template <bool FINAL>
__global__ __launch_bounds__(256, 1) void k_g2(u8* __restrict__ ws, const u8* __restrict__ b2, u8* __restrict__ out) {
	__shared__ __attribute__((aligned(16))) u8 sm[65536 + (FINAL ? 57344 : 1024)];
	const int tid = threadIdx.x, lane = tid & 63, w = __builtin_amdgcn_readfirstlane(tid >> 6);
	const int b = blockIdx.x, slot = b >> 3, pt = 4 * (b & 7) + slot / 7, jt = slot % 7;
	const int h = lane >> 5, pq = tid & 15, jr = tid >> 4;
	f32x16 acc[2][2];
#pragma unroll
	for (int i = 0; i < 4; ++i)
#pragma unroll
		for (int r = 0; r < 16; ++r) acc[i >> 1][i & 1][r] = 0.f;
	const u8* hp = ws + (FINAL ? O_H2 : O_H1) + (2 * pt * 112 + 28 * w) * 1024 + lane * 16;
	const u8* wp = ws + (FINAL ? O_W2O : O_W2F) + (2 * jt * 112 + 28 * w) * 1024 + lane * 16;
	u32x2 res[4];
	float bias[4];
	if (FINAL) {
		u8* st = sm + 65536 + w * 14336;
		rsrc_t rs = mkrs(ws + O_XT3, XT_B);
		unsigned vo = (lane >> 3) * 4096 + (((lane & 7) ^ (((lane >> 4) & 1) << 2)) * 16);
#pragma unroll
		for (int n = 0; n < 14; ++n) dma16(rs, st + n * 1024, vo, (112 * w + 8 * n) * 4096 + pt * 128);
		const u8* qp = ws + O_QT + (2 * jt * 28 + 7 * w) * 1024 + lane * 16;
		u32x4 qa[2][7];
#pragma unroll
		for (int n = 0; n < 7; ++n) { qa[0][n] = ldg16(qp + n * 1024); qa[1][n] = ldg16(qp + (28 + n) * 1024); }
		u32x2 bb = *(const u32x2*)(b2 + (64 * jt + 4 * pq) * 2);
		bias[0] = bflo(bb[0]); bias[1] = bfhi(bb[0]); bias[2] = bflo(bb[1]); bias[3] = bfhi(bb[1]);
		asm volatile("s_waitcnt vmcnt(0)" ::: "memory");
		const int q = (lane >> 2) & 3, pc = lane & 3, cg = (lane >> 4) & 1;
		const u8* rb = st + (8 * h + q) * 128 + (2 * cg + (pc >> 1)) * 16 + (pc & 1) * 8;
#pragma unroll
		for (int n = 0; n < 7; ++n)
#pragma unroll
			for (int bi = 0; bi < 2; ++bi) {
				const u8* p = rb + ((bi ^ (q >> 1)) * 64) + n * 2048;
				u32x4 bf = cat(trread(p), trread(p + 512));
				acc[0][bi] = mfma32(qa[0][n], bf, acc[0][bi]);
				acc[1][bi] = mfma32(qa[1][n], bf, acc[1][bi]);
			}
	} else {
#pragma unroll
		for (int i = 0; i < 4; ++i) {
			res[i] = *(const u32x2*)(ws + O_XT1 + (64 * jt + jr + 16 * i) * 4096 + pt * 128 + pq * 8);
			bias[i] = ((const float*)(ws + O_B2H))[64 * jt + jr + 16 * i];
		}
	}
#pragma unroll
	for (int n = 0; n < 28; ++n) {
		u32x4 h0 = ldg16(hp + n * 1024), h1 = ldg16(hp + (112 + n) * 1024);
		u32x4 w0 = ldg16(wp + n * 1024), w1 = ldg16(wp + (112 + n) * 1024);
		if (FINAL) {
			acc[0][0] = mfma32(w0, h0, acc[0][0]);
			acc[0][1] = mfma32(w0, h1, acc[0][1]);
			acc[1][0] = mfma32(w1, h0, acc[1][0]);
			acc[1][1] = mfma32(w1, h1, acc[1][1]);
		} else {
			acc[0][0] = mfma32(h0, w0, acc[0][0]);
			acc[0][1] = mfma32(h0, w1, acc[0][1]);
			acc[1][0] = mfma32(h1, w0, acc[1][0]);
			acc[1][1] = mfma32(h1, w1, acc[1][1]);
		}
	}
#pragma unroll
	for (int a = 0; a < 2; ++a)
#pragma unroll
		for (int bi = 0; bi < 2; ++bi)
#pragma unroll
			for (int q = 0; q < 4; ++q) {
				int row = 32 * bi + (lane & 31);
				*(f32x4*)(sm + w * 16384 + row * 256 + (((8 * a + 2 * q + h) ^ (lane & 7)) * 16)) =
					f32x4{acc[a][bi][4 * q], acc[a][bi][4 * q + 1], acc[a][bi][4 * q + 2], acc[a][bi][4 * q + 3]};
			}
	__syncthreads();
	f32x4 ssv = {0.f, 0.f, 0.f, 0.f};
#pragma unroll
	for (int i = 0; i < 4; ++i) {
		const int row = jr + 16 * i;
		const u8* p = sm + row * 256 + ((pq ^ (row & 7)) * 16);
		f32x4 v = (*(const f32x4*)p + *(const f32x4*)(p + 16384)) + (*(const f32x4*)(p + 32768) + *(const f32x4*)(p + 49152));
		if (FINAL) {
			*(u32x2*)(out + (32 * row + pt) * 896 + jt * 128 + pq * 8) = u32x2{pack2(v[0] + bias[0], v[1] + bias[1]), pack2(v[2] + bias[2], v[3] + bias[3])};
		} else {
			v[0] += bias[i] + bflo(res[i][0]); v[1] += bias[i] + bfhi(res[i][0]);
			v[2] += bias[i] + bflo(res[i][1]); v[3] += bias[i] + bfhi(res[i][1]);
			*(u32x2*)(ws + O_XT2 + (64 * jt + row) * 4096 + pt * 128 + pq * 8) = u32x2{pack2(v[0], v[1]), pack2(v[2], v[3])};
			if (!(i == 0 && 64 * jt + jr == 1)) ssv += v * v;
		}
	}
	if (!FINAL) {
#pragma unroll
		for (int e = 0; e < 4; ++e) {
			ssv[e] += __shfl_xor(ssv[e], 16);
			ssv[e] += __shfl_xor(ssv[e], 32);
		}
		if (lane < 16) *(f32x4*)(sm + 65536 + w * 256 + lane * 16) = ssv;
		__syncthreads();
		if (tid < 64) {
			const float* p = (const float*)(sm + 65536) + tid;
			((float*)(ws + O_SS))[jt * 2048 + 64 * pt + tid] = (p[0] + p[64]) + (p[128] + p[192]);
		}
	}
}

extern "C" void kernel_launch(void* const* d_in, const int* in_sizes, int n_in, void* d_out, int out_size, void* d_ws, size_t ws_size,
		hipStream_t stream) {
	u8* ws = (u8*)d_ws;
	k_p0<<<256, 256, 0, stream>>>((const u8*)d_in[0], (const u16*)d_in[1], (const u16*)d_in[2], (const u8*)d_in[4], (const u8*)d_in[6],
		(const u16*)d_in[7], ws);
	k_col<true><<<224, 256, 0, stream>>>(ws);
	k_g1<true><<<224, 256, 0, stream>>>(ws, (const u8*)d_in[5]);
	k_g2<false><<<224, 256, 0, stream>>>(ws, (const u8*)d_in[7], (u8*)d_out);
	k_col<false><<<224, 256, 0, stream>>>(ws);
	k_g1<false><<<224, 256, 0, stream>>>(ws, (const u8*)d_in[5]);
	k_g2<true><<<224, 256, 0, stream>>>(ws, (const u8*)d_in[7], (u8*)d_out);
}
```

```cpp
#include <hip/hip_runtime.h>
#include <hip/hip_bf16.h>
#include <stdint.h>

typedef __bf16 bf16x8 __attribute__((ext_vector_type(8)));
typedef __bf16 bf16x2 __attribute__((ext_vector_type(2)));
typedef short s16x4 __attribute__((ext_vector_type(4)));
typedef float f32x2 __attribute__((ext_vector_type(2)));
typedef float f32x4 __attribute__((ext_vector_type(4)));
typedef float f32x16 __attribute__((ext_vector_type(16)));
typedef unsigned u32x2 __attribute__((ext_vector_type(2)));
typedef unsigned u32x4 __attribute__((ext_vector_type(4)));
typedef u32x4 u32x4u __attribute__((aligned(2)));
typedef unsigned char u8;
typedef unsigned short u16;
typedef __amdgpu_buffer_rsrc_t rsrc_t;
#define LAS __attribute__((address_space(3)))
#define DEV __device__ __forceinline__

constexpr unsigned STRIP = 28672u;
constexpr unsigned XT_B = 64u * STRIP;
constexpr unsigned FR_B = 1605632u;
constexpr unsigned H_B = 7340032u;
constexpr unsigned O_FLAG0 = 0, O_FLAG1 = 32768, O_FLAGP = 61440, O_EPOCH = 65536;
constexpr unsigned O_B2 = 66560;
constexpr unsigned O_SS0 = 70656, O_SS1 = 74752;
constexpr unsigned O_XT0 = 131072;
constexpr unsigned O_XT1 = O_XT0 + XT_B;
constexpr unsigned O_XT2 = O_XT1 + XT_B;
constexpr unsigned O_H1 = O_XT2 + XT_B;
constexpr unsigned O_XT3 = O_H1 + H_B;
constexpr unsigned O_H2 = O_XT3 + XT_B;
constexpr unsigned O_W1F = O_H2 + H_B;
constexpr unsigned O_W2F = O_W1F + FR_B;
constexpr unsigned O_W2O = O_W2F + FR_B;
constexpr unsigned O_QT = O_W2O + FR_B;
constexpr unsigned O_WT = O_QT + 401408;
constexpr unsigned WS_END = O_WT + XT_B;
constexpr unsigned L_TOP = 114688;
constexpr unsigned L_TAB = L_TOP + 48640;

constexpr float EPS = 1e-5f;
constexpr float SC1 = 0.04724555912615340f;
constexpr float SC2 = 0.06681531047810609f;
constexpr unsigned POIS = 0xffffffffu;
#define SPINB 18
constexpr int SPIN = 1 << SPINB;

DEV unsigned pack2(float a, float b) {
	f32x2 v = {a, b};
	return __builtin_bit_cast(unsigned, __builtin_convertvector(v, bf16x2));
}
DEV unsigned pack2c(float a, float b) { return min(pack2(a, b), 0xfffffffeu); }
DEV float bflo(unsigned u) { return __builtin_bit_cast(float, u << 16); }
DEV float bfhi(unsigned u) { return __builtin_bit_cast(float, u & 0xffff0000u); }
DEV f32x4 mfma16(u32x4 a, u32x4 b, f32x4 c) {
	return __builtin_amdgcn_mfma_f32_16x16x32_bf16(__builtin_bit_cast(bf16x8, a), __builtin_bit_cast(bf16x8, b), c, 0, 0, 0);
}
DEV f32x16 mfma32(u32x4 a, u32x4 b, f32x16 c) {
	return __builtin_amdgcn_mfma_f32_32x32x16_bf16(__builtin_bit_cast(bf16x8, a), __builtin_bit_cast(bf16x8, b), c, 0, 0, 0);
}
DEV rsrc_t mkrs(const void* p, unsigned bytes) { return __builtin_amdgcn_make_buffer_rsrc((void*)p, 0, bytes, 0x00020000); }
DEV void dma16(rsrc_t rs, u8* lds, unsigned voff, unsigned soff) {
	__builtin_amdgcn_raw_ptr_buffer_load_lds(rs, (LAS void*)lds, 16, voff, soff, 0, 0);
}
DEV u32x4 ld16(rsrc_t rs, unsigned v, unsigned s) { return __builtin_amdgcn_raw_buffer_load_b128(rs, v, s, 16); }
DEV u32x2 ld8(rsrc_t rs, unsigned v, unsigned s) { return __builtin_amdgcn_raw_buffer_load_b64(rs, v, s, 16); }
DEV unsigned ld4(rsrc_t rs, unsigned v, unsigned s) { return __builtin_amdgcn_raw_buffer_load_b32(rs, v, s, 16); }
DEV void st16(rsrc_t rs, u32x4 d, unsigned v, unsigned s) { __builtin_amdgcn_raw_buffer_store_b128(d, rs, v, s, 16); }
DEV void st8(rsrc_t rs, u32x2 d, unsigned v, unsigned s) { __builtin_amdgcn_raw_buffer_store_b64(d, rs, v, s, 16); }
DEV void st4(rsrc_t rs, unsigned d, unsigned v, unsigned s) { __builtin_amdgcn_raw_buffer_store_b32(d, rs, v, s, 16); }
DEV u32x2 trread(const u8* lds) {
	return __builtin_bit_cast(u32x2, __builtin_amdgcn_ds_read_tr16_b64_v4i16((LAS s16x4*)lds));
}
DEV u32x4 cat(u32x2 a, u32x2 b) { return u32x4{a[0], a[1], b[0], b[1]}; }
DEV float cosr(float x) { return __builtin_amdgcn_cosf(x); }
DEV float sinr(float x) { return __builtin_amdgcn_sinf(x); }
DEV unsigned mx4(unsigned m, u32x4 v) { return max(max(max(m, v[0]), v[1]), max(max(v[2], v[3]), 0u)); }
DEV bool anyp(unsigned m) { return __builtin_amdgcn_ballot_w64(m == POIS) != 0; }
DEV void swap32(u32x4& a, u32x4& b) {
#pragma unroll
	for (int i = 0; i < 4; ++i) {
		auto r = __builtin_amdgcn_permlane32_swap(a[i], b[i], false, false);
		a[i] = r[0]; b[i] = r[1];
	}
}
DEV void swap16(u32x4& a, u32x4& b) {
#pragma unroll
	for (int i = 0; i < 4; ++i) {
		auto r = __builtin_amdgcn_permlane16_swap(a[i], b[i], false, false);
		a[i] = r[0]; b[i] = r[1];
	}
}
#define WAIT_BAR(N) asm volatile("s_waitcnt vmcnt(" #N ") lgkmcnt(0)\n\ts_barrier" ::: "memory")
#define LBAR() asm volatile("s_waitcnt lgkmcnt(0)\n\ts_barrier" ::: "memory")
#define FENCE() asm volatile("" ::: "memory")
#define PIN(x) asm volatile("" : "+v"(x))
#define SB() __builtin_amdgcn_sched_barrier(0)
#define UNLIKELY(x) __builtin_expect(!!(x), 0)

DEV void poll_flag(rsrc_t rs, bool act, unsigned v, unsigned s, u32x2 tag) {
	bool ok = !act;
	for (int it = 0; it < SPIN; ++it) {
		if (!ok) {
			u32x2 a = ld8(rs, v, s);
			ok = (a[0] == tag[0]) & (a[1] == tag[1]);
		}
		if (__builtin_amdgcn_ballot_w64(!ok) == 0) break;
		__builtin_amdgcn_s_sleep(2);
		FENCE();
	}
	FENCE();
}

DEV void basis(int j, float& kq, float& ph) {
	int c = j >> 1;
	int kk = c ? c : ((j & 1) ? 0 : 224);
	kq = (float)kk * (1.0f / 448.0f);
	ph = (c && (j & 1)) ? 0.25f : 0.0f;
}

DEV void p0_rows_gemm(const u8* sm, const u32x4 (&qf)[14], f32x4 (&acc)[4], int g, int i16) {
	unsigned sg = 2 * (i16 >> 2) + ((i16 >> 1) & 1);
	const u8* base = sm + (8 * (i16 >> 2) + (i16 & 3)) * 896;
	const u8* b0 = base + ((g ^ sg) * 16);
	const u8* b1 = base + (((4 + g) ^ sg) * 16);
#pragma unroll
	for (int t = 0; t < 4; ++t) acc[t] = f32x4{0.f, 0.f, 0.f, 0.f};
#pragma unroll
	for (int ks = 0; ks < 14; ++ks) {
#pragma unroll
		for (int t = 0; t < 4; ++t) {
			u32x4 a = *(const u32x4*)(((ks & 1) ? b1 : b0) + (ks >> 1) * 128 + (t >> 1) * 28672 + (t & 1) * 3584);
			acc[t] = mfma16(a, qf[ks], acc[t]);
		}
	}
}

struct ColK {
	u32x4 C1[2], S1[2], C2[2], S2[2];
	float twc[8], tws[8], tic[8], tis[8];
};
DEV void col_consts(ColK& K, int w, int lane) {
	PIN(lane);
	const int g = lane >> 4, i16 = lane & 15;
	const float f1 = (float)(16 * w + i16);
#pragma unroll
	for (int ks = 0; ks < 2; ++ks)
#pragma unroll
		for (int i = 0; i < 4; ++i) {
			float a = f1 * (float)(32 * ks + 8 * g + 2 * i) * (1.0f / 64.0f), bb = a + f1 * (1.0f / 64.0f);
			K.C1[ks][i] = pack2(cosr(a), cosr(bb));
			K.S1[ks][i] = pack2(sinr(a), sinr(bb));
		}
#pragma unroll
	for (int t2 = 0; t2 < 2; ++t2)
#pragma unroll
		for (int i = 0; i < 4; ++i) {
			float m = (float)(16 * t2 + i16) * (1.0f / 32.0f);
			float a = m * (float)(16 * (i >> 1) + 4 * g + 2 * (i & 1)), bb = a + m;
			K.C2[t2][i] = pack2(cosr(a), cosr(bb));
			K.S2[t2][i] = pack2(sinr(a), sinr(bb));
		}
#pragma unroll
	for (int i = 0; i < 8; ++i) {
		float a = f1 * (float)(16 * (i >> 2) + 4 * g + (i & 3)) * (1.0f / 2048.0f);
		K.twc[i] = cosr(a);
		K.tws[i] = sinr(a);
		float bb = (float)(16 * w + 4 * g + (i & 3)) * (float)(16 * (i >> 2) + i16) * (1.0f / 2048.0f);
		K.tic[i] = cosr(bb) * (1.0f / 2048.0f);
		K.tis[i] = sinr(bb) * (1.0f / 2048.0f);
	}
#pragma unroll
	for (int i = 0; i < 2; ++i) { PIN(K.C1[i]); PIN(K.S1[i]); PIN(K.C2[i]); PIN(K.S2[i]); }
#pragma unroll
	for (int i = 0; i < 8; ++i) { PIN(K.twc[i]); PIN(K.tws[i]); PIN(K.tic[i]); PIN(K.tis[i]); }
}

DEV void gelu_store(const f32x16& acc, float r, const u8* tab, rsrc_t rs, unsigned v, unsigned s) {
	unsigned o[8];
#pragma unroll
	for (int q = 0; q < 4; ++q) {
		const f32x4 bb = *(const f32x4*)(tab + q * 32);
		float y[4];
#pragma unroll
		for (int e = 0; e < 4; ++e) {
			float pre = __builtin_fmaf(acc[4 * q + e], r, bb[e]);
			float z = pre * __builtin_fmaf(pre * pre, -0.10294324f, -2.30220820f);
			y[e] = pre * __builtin_amdgcn_rcpf(1.0f + __builtin_amdgcn_exp2f(z));
		}
		o[2 * q] = pack2c(y[0], y[1]);
		o[2 * q + 1] = pack2c(y[2], y[3]);
	}
	st16(rs, u32x4{o[0], o[1], o[2], o[3]}, v, s);
	st16(rs, u32x4{o[4], o[5], o[6], o[7]}, v + 1024, s);
}
template <int CTRL> DEV float dpp(float x) {
	return __builtin_bit_cast(float, __builtin_amdgcn_update_dpp(0, __builtin_bit_cast(int, x), CTRL, 0xf, 0xf, true));
}

#define N2 4
#define N3 9
#define D_S3 0
#define D_S4 16

template <int HI> DEV void p0_gemm(const u8* sm, const u32x4 (&qf)[14], f32x4 (&acc)[4], int g, int i16) {
	unsigned sg = 2 * (i16 >> 2) + ((i16 >> 1) & 1);
	const u8* base = sm + (8 * (i16 >> 2) + (i16 & 3)) * 896;
	const u8* b0 = base + ((g ^ sg) * 16);
	const u8* b1 = base + (((4 + g) ^ sg) * 16);
#pragma unroll
	for (int t = 0; t < 4; ++t) acc[t] = f32x4{0.f, 0.f, 0.f, 0.f};
#pragma unroll
	for (int ks = 0; ks < 14; ++ks) {
#pragma unroll
		for (int t = 0; t < 4; ++t) {
			u32x4 a = *(const u32x4*)(((ks & 1) ? b1 : b0) + (ks >> 1) * 128 + (t >> 1) * HI + (t & 1) * 3584);
			acc[t] = mfma16(a, qf[ks], acc[t]);
		}
	}
}
DEV float dot2(unsigned a, unsigned b, float c) {
	return __builtin_amdgcn_fdot2_f32_bf16(__builtin_bit_cast(bf16x2, a), __builtin_bit_cast(bf16x2, b), c, false);
}

__global__ __launch_bounds__(256, 1) void k_all(const u8* __restrict__ x, const u8* __restrict__ wr, const u8* __restrict__ wi,
		const u8* __restrict__ w1, const u8* __restrict__ b1, const u8* __restrict__ w2, const u16* __restrict__ b2,
		u8* out, u8* ws) {
	__shared__ __attribute__((aligned(16))) u8 sm[163840];
	const int tid = threadIdx.x, lane = tid & 63, w = __builtin_amdgcn_readfirstlane(tid >> 6);
	const int g = lane >> 4, i16 = lane & 15, h = lane >> 5;
	const int q4 = (lane >> 2) & 3, pc = lane & 3;
	const int b = blockIdx.x;
	const rsrc_t rs = mkrs(ws, WS_END);
	const u32x4 pois = {POIS, POIS, POIS, POIS};
	if (b >= 224) {
		const int sb = b - 224;
		const u32x2 ep = ld8(rs, 0, O_EPOCH);
		const int li = lane & 31;
		u32x4 v[16];
		if (li < 29) {
			const u8* src = ((w >> 1) ? wi : wr) + (2 * sb + (w & 1) + 64 * h) * 450 + (li < 28 ? 16 * li : 434);
#pragma unroll
			for (int n = 0; n < 16; ++n) v[n] = *(const u32x4u*)(src + n * (128 * 450));
		}
		for (int jj = w; jj < 14; jj += 4) {
			int j = sb * 14 + jj;
			float kq, ph, s = 0.f;
			basis(j, kq, ph);
#pragma unroll
			for (int i = 0; i < 7; ++i) {
				int d = lane + 64 * i;
				s = __builtin_fmaf(bflo(b2[d]), cosr(__builtin_fmaf(kq, (float)d, ph)), s);
			}
#pragma unroll
			for (int m = 1; m < 64; m <<= 1) s += __shfl_xor(s, m);
			if (lane == 0) {
				st4(rs, __builtin_bit_cast(unsigned, s * ((j < 2) ? SC1 : SC2)), j * 4, O_B2);
				st4(rs, (unsigned)b2[j] << 16, j * 4, O_B2 + 1792);
			}
		}
		if (li < 29) {
			u8* dst = sm + (32 * w + h) * 464;
#pragma unroll
			for (int n = 0; n < 16; ++n) {
				*(u32x4*)(dst + n * 928 + li * 16) = v[n];
				if (li == 28) *(u16*)(dst + n * 928) = (u16)(v[n][3] >> 16);
			}
		}
		LBAR();
#pragma unroll
		for (int n = 0; n < 7; ++n) {
			const int task = w + 4 * n, cgp = task >> 1, fl = task & 1;
#pragma unroll
			for (int t = 0; t < 2; ++t) {
				const u8* p = sm + (fl * 32 + 16 * t + 4 * g + q4) * 464 + cgp * 32 + pc * 8;
				u32x2 re = trread(p), im = trread(p + 64 * 464);
				u32x4 o = {__builtin_amdgcn_perm(im[0], re[0], 0x05040100u), __builtin_amdgcn_perm(im[0], re[0], 0x07060302u),
						   __builtin_amdgcn_perm(im[1], re[1], 0x05040100u), __builtin_amdgcn_perm(im[1], re[1], 0x07060302u)};
				st16(rs, o, i16 * 8192 + g * 16 + t * 64, O_WT + cgp * 131072 + (2 * sb + fl) * 128);
			}
		}
		const unsigned long long tg = (((unsigned long long)ep[1] << 32) | ep[0]) + 0x9e3779b97f4a7c15ull;
		const u32x2 tag = {(unsigned)tg, (unsigned)(tg >> 32)};
		WAIT_BAR(0);
		if (tid == 0) st8(rs, tag, 0, O_FLAG0 + b * 128);
		for (int fr = sb * 4 + w; fr < 392; fr += 128) {
			int dt = fr / 28, ks = fr % 28;
			float d = (float)(32 * dt + li);
			float c[8];
#pragma unroll
			for (int i = 0; i < 8; ++i) {
				int j = 16 * ks + 8 * h + i;
				float kq, ph;
				basis(j, kq, ph);
				c[i] = cosr(__builtin_fmaf(kq, d, ph)) * ((j < 2) ? SC1 : SC2);
			}
			st16(rs, u32x4{pack2(c[0], c[1]), pack2(c[2], c[3]), pack2(c[4], c[5]), pack2(c[6], c[7])}, lane * 16, O_QT + fr * 1024);
		}
#pragma unroll 2
		for (int n = 0; n < 70; ++n) st16(rs, pois, tid * 16, O_XT3 + (sb * 70 + n) * 4096);
		WAIT_BAR(0);
		if (tid == 0) st8(rs, tag, 0, O_FLAGP + sb * 128);
		return;
	}
	const int xcd = b & 7, slot = b >> 3, s7 = slot / 7, jt = slot % 7;
	ColK K;
	u32x2 tag;
	{
		const int pt = 4 * xcd + s7;
		const bool fold = pt < 28;
		u8* bufW = sm + 57344;
		if (lane < 56) {
			const rsrc_t rx = mkrs(x, 2048u * 896u);
#pragma unroll
			for (int n = 0; n < 16; ++n) {
				const int r = 32 * (n >> 3) + 8 * w + (n & 7);
				dma16(rx, sm + r * 896, ((unsigned)lane ^ (2 * w + ((n >> 1) & 1))) * 16, pt * 896u + r * 28672u);
			}
		}
		u32x4 w2r[8];
		const unsigned w1f_o = O_W1F + (2 * pt * 28 + 4 * jt + w) * 1024;
		if (fold) {
			const rsrc_t r1 = mkrs(w1, 448u * 3584u);
			unsigned v0 = (lane >> 3) * 3584 + (((lane & 7) ^ (((lane >> 4) & 1) << 1)) * 16);
			unsigned v1 = (lane >> 3) * 3584 + (((lane & 7) ^ ((((lane >> 4) & 1) | 2) << 1)) * 16);
#pragma unroll
			for (int n = 0; n < 14; ++n) {
				int i = 14 * w + n;
				dma16(r1, bufW + i * 1024, (n & 1) ? v1 : v0, i * (8 * 3584) + pt * 128);
			}
			if (lane < 56) {
				const rsrc_t r2 = mkrs(w2, 1792u * 896u);
#pragma unroll
				for (int n = 0; n < 8; ++n)
					dma16(r2, sm + L_TOP + (8 * w + n) * 896, ((unsigned)lane ^ (2 * w + ((n >> 1) & 1))) * 16, (64 * pt + 16 * w + n) * 896u);
#pragma unroll
				for (int n = 0; n < 8; ++n) w2r[n] = __builtin_amdgcn_raw_buffer_load_b128(r2, lane * 16, (64 * pt + 16 * w + 8 + n) * 896u, 0);
			}
			st16(rs, pois, lane * 16, w1f_o);
			st16(rs, pois, lane * 16, w1f_o + 28 * 1024);
		}
		const u32x2 ep = ld8(rs, 0, O_EPOCH);
#pragma unroll
		for (int n = 0; n < 12; ++n) st16(rs, pois, tid * 16, O_XT1 + (b * 12 + n) * 4096);
		if (lane < 8) st16(rs, pois, lane * 16, O_SS1 + b * 128);
		SB();
		const int j = 64 * jt + 16 * w + i16;
		float kq, ph;
		basis(j, kq, ph);
		const float a0 = (float)(8 * g) * kq + ph;
		u32x4 qf[14];
#pragma unroll
		for (int ks = 0; ks < 14; ++ks) {
#pragma unroll
			for (int i = 0; i < 4; ++i)
				qf[ks][i] = pack2(cosr(__builtin_fmaf(kq, (float)(32 * ks + 2 * i), a0)), cosr(__builtin_fmaf(kq, (float)(32 * ks + 2 * i + 1), a0)));
			PIN(qf[ks]);
		}
		col_consts(K, w, lane);
		const float scj = (j < 2) ? SC1 : SC2;
		if (fold) WAIT_BAR(46); else WAIT_BAR(14);
		f32x4 acc[4];
		p0_gemm<28672>(sm, qf, acc, g, i16);
		float ssr = 0.f;
		if (jt < 4) {
			const u8* rb = sm + (16 * jt + 4 * w + g) * 896 + i16 * 16;
			float s1 = 0.f, s2 = 0.f;
#pragma unroll
			for (int q = 0; q < 4; ++q) {
				u32x4 v = {0u, 0u, 0u, 0u};
				if (q < 3 || i16 < 8) v = *(const u32x4*)(rb + q * 256);
#pragma unroll
				for (int i = 0; i < 4; ++i) {
					s1 = dot2(v[i], 0x3f803f80u, s1);
					s2 = dot2(v[i], v[i], s2);
				}
			}
			s1 += dpp<0x128>(s1); s2 += dpp<0x128>(s2);
			s1 += dpp<0x124>(s1); s2 += dpp<0x124>(s2);
			s1 += dpp<0x122>(s1); s2 += dpp<0x122>(s2);
			s1 += dpp<0x121>(s1); s2 += dpp<0x121>(s2);
			ssr = fmaxf(s2 - s1 * s1 * (1.0f / 448.0f), 0.f);
		}
		WAIT_BAR(0);
#pragma unroll
		for (int u = 0; u < 2; ++u) {
			u32x4 o = {pack2(acc[2 * u][0] * scj, acc[2 * u][1] * scj), pack2(acc[2 * u][2] * scj, acc[2 * u][3] * scj),
					   pack2(acc[2 * u + 1][0] * scj, acc[2 * u + 1][1] * scj), pack2(acc[2 * u + 1][2] * scj, acc[2 * u + 1][3] * scj)};
			st16(rs, o, i16 * 64 + g * 16, O_XT0 + (2 * pt + u) * STRIP + (64 * jt + 16 * w) * 64);
		}
		if (jt < 4) {
			int sv = __builtin_bit_cast(int, ssr);
			float t0 = __builtin_bit_cast(float, __builtin_amdgcn_readlane(sv, 0)), t1 = __builtin_bit_cast(float, __builtin_amdgcn_readlane(sv, 16));
			float t2 = __builtin_bit_cast(float, __builtin_amdgcn_readlane(sv, 32)), t3 = __builtin_bit_cast(float, __builtin_amdgcn_readlane(sv, 48));
			if (lane == 0) st8(rs, u32x2{pack2(t0, t1), pack2(t2, t3)}, 0, O_SS0 + (64 * pt + 16 * jt + 4 * w) * 2);
		}
		const unsigned long long tg = (((unsigned long long)ep[1] << 32) | ep[0]) + 0x9e3779b97f4a7c15ull;
		tag = u32x2{(unsigned)tg, (unsigned)(tg >> 32)};
		if (fold) {
			if (lane < 56) {
#pragma unroll
				for (int n = 0; n < 8; ++n) *(u32x4*)(sm + (8 * w + n) * 896 + ((lane ^ (2 * w + ((n >> 1) & 1))) * 16)) = w2r[n];
			}
			const unsigned kap = ((q4 >> 1) & 1) | ((g & 1) << 1);
			const u8* rb = bufW + (8 * g + q4) * 128 + (pc & 1) * 8;
#pragma unroll
			for (int t = 0; t < 4; ++t) acc[t] = f32x4{0.f, 0.f, 0.f, 0.f};
#pragma unroll
			for (int ks = 0; ks < 14; ++ks) {
#pragma unroll
				for (int t = 0; t < 4; ++t) {
					const u8* p = rb + (((2 * t + (pc >> 1)) ^ (kap << 1)) * 16) + ks * 4096;
					acc[t] = mfma16(qf[ks], cat(trread(p), trread(p + 512)), acc[t]);
				}
			}
			const int j0 = 64 * jt + 16 * w + 4 * g;
			const float s0 = (j0 == 0) ? SC1 : SC2, s1 = (j0 == 0) ? 0.f : SC2;
#pragma unroll
			for (int u = 0; u < 2; ++u) {
				unsigned x0 = pack2c(acc[2 * u][0] * s0, acc[2 * u][1] * s1), x1 = pack2c(acc[2 * u][2] * SC2, acc[2 * u][3] * SC2);
				unsigned y0 = pack2c(acc[2 * u + 1][0] * s0, acc[2 * u + 1][1] * s1), y1 = pack2c(acc[2 * u + 1][2] * SC2, acc[2 * u + 1][3] * SC2);
				auto r0 = __builtin_amdgcn_permlane16_swap(x0, y0, false, false);
				auto r1 = __builtin_amdgcn_permlane16_swap(x1, y1, false, false);
				st16(rs, u32x4{r0[0], r1[0], r0[1], r1[1]}, lane * 16, w1f_o + u * 28 * 1024);
			}
			WAIT_BAR(2);
		} else {
			WAIT_BAR(0);
		}
		if (tid == 0) st8(rs, tag, 0, O_FLAG0 + b * 128);
		if (fold) {
			p0_gemm<-(int)L_TOP>(sm + L_TOP, qf, acc, g, i16);
			const int kk = 4 * pt + g;
			const unsigned kv = (28 * ((kk >> 1) & 3) + 2 * (kk >> 3) + (kk & 1)) * 1024 + (16 * (w & 1) + i16) * 16;
#pragma unroll
			for (int hh = 0; hh < 2; ++hh) {
				u32x4 o = {pack2(acc[hh][0] * scj, acc[hh][1] * scj), pack2(acc[hh][2] * scj, acc[hh][3] * scj),
						   pack2(acc[2 + hh][0] * scj, acc[2 + hh][1] * scj), pack2(acc[2 + hh][2] * scj, acc[2 + hh][3] * scj)};
				st16(rs, o, kv + hh * 512, O_W2F + (2 * jt + (w >> 1)) * 112 * 1024);
			}
			const int kw = 4 * pt + w;
			const unsigned ko = (28 * ((kw >> 1) & 3) + 2 * (kw >> 3) + (kw & 1)) * 1024;
			const unsigned sg = 2 * w + ((q4 >> 1) & 1);
#pragma unroll
			for (int u = 0; u < 2; ++u) {
				unsigned ch = 8 * jt + ((4 * u + 2 * (g & 1) + (pc >> 1)) ^ sg);
				const u8* p = sm + (8 * w + 4 * h + q4) * 896 + ch * 16 + (pc & 1) * 8;
				st16(rs, cat(trread(p + L_TOP), trread(p)), lane * 16, O_W2O + (2 * jt + u) * 112 * 1024 + ko);
			}
		}
	}
	const int pp = 4 * (xcd >> 1) + s7, ft = 7 * (xcd & 1) + jt, pt = 2 * pp + (xcd & 1);
	u32x4 wv[2];
#pragma unroll
	for (int l = 0; l < 2; ++l) {
		int tid = threadIdx.x;
		PIN(tid);
		const int lane = tid & 63, g = lane >> 4, i16 = lane & 15, h = lane >> 5, q4 = (lane >> 2) & 3, pc = lane & 3;
		const unsigned o_ci = l ? O_XT2 : O_XT0, o_co = l ? O_XT3 : O_XT1, o_h = l ? O_H2 : O_H1;
		const unsigned o_w0 = (l ? O_W2O : O_W2F) + ((2 * jt + (w & 1)) * 112 + 7 * w) * 1024;
		const unsigned o_w1 = (l ? O_W2O : O_W2F) + ((2 * jt + ((w & 1) ^ 1)) * 112 + 7 * w) * 1024;
		u32x4 W[28][2];
		u32x4 w1s[28];
		{
			if (l == 0) {
				poll_flag(rs, true, tid * 128, O_FLAG0, tag);
				LBAR();
				if (b == 0 && tid == 0) st8(rs, tag, 0, O_EPOCH);
			} else {
				if (D_S3) __builtin_amdgcn_s_sleep(D_S3);
				poll_flag(rs, lane < 32, lane * 128, O_FLAGP, tag);
			}
			const unsigned xo = (16 * w + ((lane & 31) >> 2)) * STRIP + h * 64 + pc * 16;
			u32x4 xr = ld16(rs, xo, o_ci + b * 128), xi = ld16(rs, xo, o_ci + b * 128 + 8 * STRIP);
			u32x4 ssp[7];
			if (l == 0) {
				ssp[0] = ld16(rs, tid * 16, O_SS0);
				wv[0] = ld16(rs, (16 * w + i16) * 128 + g * 16, O_WT + b * 8192);
				wv[1] = ld16(rs, (16 * w + i16) * 128 + g * 16 + 64, O_WT + b * 8192);
				SB();
#pragma unroll
				for (int n = 0; n < 28; ++n) w1s[n] = ld16(rs, lane * 16, O_W1F + ((4 * ft + w) * 28 + n) * 1024);
			} else {
#pragma unroll
				for (int k = 0; k < 7; ++k) ssp[k] = ld16(rs, tid * 16, O_SS1 + k * 4096);
				SB();
#pragma unroll
				for (int n = 0; n < 14; ++n) {
					W[n][0] = ld16(rs, lane * 16, o_w0 + ((n / 7) * 28 + n % 7) * 1024);
					W[n][1] = ld16(rs, lane * 16, o_w1 + ((n / 7) * 28 + n % 7) * 1024);
				}
			}
			SB();
			if (l == 1) {
				unsigned m = mx4(mx4(0, xr), xi);
#pragma unroll
				for (int k = 0; k < 7; ++k) m = mx4(m, ssp[k]);
				if (UNLIKELY(anyp(m))) {
					for (int it = 0; it < SPIN; ++it) {
						__builtin_amdgcn_s_sleep(2);
						FENCE();
						if (m == POIS) {
							xr = ld16(rs, xo, o_ci + b * 128); xi = ld16(rs, xo, o_ci + b * 128 + 8 * STRIP);
#pragma unroll
							for (int k = 0; k < 7; ++k) ssp[k] = ld16(rs, tid * 16, O_SS1 + k * 4096);
							m = mx4(mx4(0, xr), xi);
#pragma unroll
							for (int k = 0; k < 7; ++k) m = mx4(m, ssp[k]);
						}
						if (!anyp(m)) break;
					}
				}
			}
			swap32(xr, xi);
			float fr[8], fi[8];
			u8* cs = sm + L_TOP;
			{
				float rr[8];
#pragma unroll
				for (int i = 0; i < 4; ++i) {
					float lo = bflo(ssp[0][i]), hi = bfhi(ssp[0][i]);
					if (l == 1) {
#pragma unroll
						for (int k = 1; k < 7; ++k) { lo = dot2(ssp[k][i], 0x00003f80u, lo); hi = dot2(ssp[k][i], 0x3f800000u, hi); }
					}
					rr[2 * i] = __builtin_amdgcn_rsqf(__builtin_fmaf(lo, 1.0f / 448.0f, EPS));
					rr[2 * i + 1] = __builtin_amdgcn_rsqf(__builtin_fmaf(hi, 1.0f / 448.0f, EPS));
					fr[2 * i] = bflo(xr[i]); fr[2 * i + 1] = bfhi(xr[i]);
					fi[2 * i] = bflo(xi[i]); fi[2 * i + 1] = bfhi(xi[i]);
				}
				const unsigned imask = b ? 0xffffffffu : 0u;
				u8* p = cs + (tid >> 3) * 160 + (tid & 7) * 16;
				*(u32x4*)p = u32x4{pack2(fr[0] * rr[0], fr[1] * rr[1]), pack2(fr[2] * rr[2], fr[3] * rr[3]), pack2(fr[4] * rr[4], fr[5] * rr[5]), pack2(fr[6] * rr[6], fr[7] * rr[7])};
				*(u32x4*)(p + 5120) = u32x4{pack2(fi[0] * rr[0], fi[1] * rr[1]) & imask, pack2(fi[2] * rr[2], fi[3] * rr[3]) & imask,
											pack2(fi[4] * rr[4], fi[5] * rr[5]) & imask, pack2(fi[6] * rr[6], fi[7] * rr[7]) & imask};
			}
			LBAR();
			if (l == 0 && tid == 0) st8(rs, tag, 0, O_FLAG1 + b * 128);
			const f32x4 z4 = {0.f, 0.f, 0.f, 0.f};
			const u32x4 sgn = {0x80008000u, 0x80008000u, 0x80008000u, 0x80008000u};
			const u32x4 N1[2] = {K.S1[0] ^ sgn, K.S1[1] ^ sgn}, N2v[2] = {K.S2[0] ^ sgn, K.S2[1] ^ sgn};
			f32x4 are[2], aim[2];
			u32x4 Bre, Bim;
#pragma unroll
			for (int t = 0; t < 2; ++t) {
				are[t] = z4; aim[t] = z4;
#pragma unroll
				for (int ks = 0; ks < 2; ++ks) {
					const u8* p = cs + (16 * t + i16) * 160 + 64 * ks + 16 * g;
					u32x4 mr = *(const u32x4*)p, mi = *(const u32x4*)(p + 5120);
					are[t] = mfma16(mr, K.C1[ks], are[t]);
					aim[t] = mfma16(mi, K.C1[ks], aim[t]);
					are[t] = mfma16(mi, K.S1[ks], are[t]);
					aim[t] = mfma16(mr, N1[ks], aim[t]);
				}
			}
#pragma unroll
			for (int t = 0; t < 2; ++t)
#pragma unroll
				for (int e = 0; e < 4; e += 2) {
					int i = 4 * t + e;
					Bre[i >> 1] = pack2(are[t][e] * K.twc[i] + aim[t][e] * K.tws[i], are[t][e + 1] * K.twc[i + 1] + aim[t][e + 1] * K.tws[i + 1]);
					Bim[i >> 1] = pack2(aim[t][e] * K.twc[i] - are[t][e] * K.tws[i], aim[t][e + 1] * K.twc[i + 1] - are[t][e + 1] * K.tws[i + 1]);
				}
#pragma unroll
			for (int t = 0; t < 2; ++t) {
				are[t] = mfma16(K.C2[t], Bre, z4);
				aim[t] = mfma16(K.C2[t], Bim, z4);
				are[t] = mfma16(K.S2[t], Bim, are[t]);
				aim[t] = mfma16(N2v[t], Bre, aim[t]);
			}
#pragma unroll
			for (int t = 0; t < 2; ++t)
#pragma unroll
				for (int e = 0; e < 4; e += 2) {
					float a0 = bflo(wv[t][e]), b0 = bfhi(wv[t][e]), a1 = bflo(wv[t][e + 1]), b1v = bfhi(wv[t][e + 1]);
					Bre[2 * t + (e >> 1)] = pack2(are[t][e] * a0 - aim[t][e] * b0, are[t][e + 1] * a1 - aim[t][e + 1] * b1v);
					Bim[2 * t + (e >> 1)] = pack2(are[t][e] * b0 + aim[t][e] * a0, are[t][e + 1] * b1v + aim[t][e + 1] * a1);
				}
#pragma unroll
			for (int t = 0; t < 2; ++t) {
				are[t] = mfma16(Bre, K.C2[t], z4);
				aim[t] = mfma16(Bim, K.C2[t], z4);
				are[t] = mfma16(Bim, N2v[t], are[t]);
				aim[t] = mfma16(Bre, K.S2[t], aim[t]);
			}
#pragma unroll
			for (int t = 0; t < 2; ++t) {
				float vr[4], vi[4];
#pragma unroll
				for (int e = 0; e < 4; ++e) {
					vr[e] = are[t][e] * K.tic[4 * t + e] - aim[t][e] * K.tis[4 * t + e];
					vi[e] = aim[t][e] * K.tic[4 * t + e] + are[t][e] * K.tis[4 * t + e];
				}
				u8* p = cs + 10240 + (16 * t + i16) * 160 + 32 * w + 8 * g;
				*(u32x2*)p = u32x2{pack2(vr[0], vr[1]), pack2(vr[2], vr[3])};
				*(u32x2*)(p + 5120) = u32x2{pack2(vi[0], vi[1]), pack2(vi[2], vi[3])};
			}
			LBAR();
#pragma unroll
			for (int t = 0; t < 2; ++t) {
				are[t] = z4; aim[t] = z4;
#pragma unroll
				for (int ks = 0; ks < 2; ++ks) {
					const u8* p = cs + 10240 + (16 * t + i16) * 160 + 64 * ks + 16 * g;
					u32x4 mr = *(const u32x4*)p, mi = *(const u32x4*)(p + 5120);
					are[t] = mfma16(K.C1[ks], mr, are[t]);
					aim[t] = mfma16(K.C1[ks], mi, aim[t]);
					are[t] = mfma16(N1[ks], mi, are[t]);
					aim[t] = mfma16(K.S1[ks], mr, aim[t]);
				}
				u8* p = cs + 20480 + (16 * t + i16) * 272 + 64 * w + 16 * g;
				*(f32x4*)p = are[t];
				*(f32x4*)(p + 8704) = aim[t];
			}
			LBAR();
			{
				const u8* p = cs + 20480 + (tid >> 3) * 272 + (tid & 7) * 32;
				f32x4 r0 = *(const f32x4*)p, r1 = *(const f32x4*)(p + 16), i0 = *(const f32x4*)(p + 8704), i1 = *(const f32x4*)(p + 8720);
				const float im = b ? 1.0f : 0.0f;
				xr = u32x4{pack2c(fr[0] + r0[0], fr[1] + r0[1]), pack2c(fr[2] + r0[2], fr[3] + r0[3]), pack2c(fr[4] + r1[0], fr[5] + r1[1]), pack2c(fr[6] + r1[2], fr[7] + r1[3])};
				xi = u32x4{pack2c(fi[0] + im * i0[0], fi[1] + im * i0[1]), pack2c(fi[2] + im * i0[2], fi[3] + im * i0[3]),
						   pack2c(fi[4] + im * i1[0], fi[5] + im * i1[1]), pack2c(fi[6] + im * i1[2], fi[7] + im * i1[3])};
				swap32(xr, xi);
				st16(rs, xr, xo, o_co + b * 128);
				st16(rs, xi, xo, o_co + b * 128 + 8 * STRIP);
			}
			SB();
		}
		if (tid < 32) {
			u32x2 bb = *(const u32x2*)(b1 + (128 * ft + 4 * tid) * 2);
			*(f32x4*)(sm + L_TAB + tid * 16) = f32x4{bflo(bb[0]), bfhi(bb[0]), bflo(bb[1]), bfhi(bb[1])};
		}
		if (l == 0) {
			unsigned m = 0;
#pragma unroll
			for (int n = 0; n < 28; ++n) m = mx4(m, w1s[n]);
			if (UNLIKELY(anyp(m))) {
				for (int it = 0; it < SPIN; ++it) {
					__builtin_amdgcn_s_sleep(2);
					FENCE();
					m = 0;
#pragma unroll
					for (int n = 0; n < 28; ++n) { w1s[n] = ld16(rs, lane * 16, O_W1F + ((4 * ft + w) * 28 + n) * 1024); m = mx4(m, w1s[n]); }
					if (!anyp(m)) break;
				}
			}
#pragma unroll
			for (int n = 0; n < 28; ++n) *(u32x4*)(sm + (w * 28 + n) * 1024 + lane * 16) = w1s[n];
			SB();
			poll_flag(rs, lane < 28, (8 * (7 * (lane & 3) + jt) + (lane >> 2)) * 128, O_FLAG1, tag);
#pragma unroll
			for (int n = 0; n < 7; ++n) {
				W[n][0] = ld16(rs, lane * 16, o_w0 + n * 1024);
				W[n][1] = ld16(rs, lane * 16, o_w1 + n * 1024);
			}
		} else {
			if (D_S4) __builtin_amdgcn_s_sleep(D_S4);
		}
		const unsigned o_xs = o_co + (4 * pp + w) * STRIP;
		{
			bool ok = lane >= 56;
			for (int it = 0; it < SPIN; ++it) {
				if (!ok) ok = ld4(rs, (56 * w + lane) * 128 + 124, o_xs) != POIS;
				if (__builtin_amdgcn_ballot_w64(!ok) == 0) break;
				__builtin_amdgcn_s_sleep(2);
				FENCE();
			}
			LBAR();
		}
		u32x4 H[28][2];
		const unsigned o_h0 = o_h + ((2 * pt + (w >> 1)) * 112 + 7 * w) * 1024, o_h1 = o_h + ((2 * pt + ((w >> 1) ^ 1)) * 112 + 7 * w) * 1024;
		{
			u32x4 xs[28], B[28];
#pragma unroll
			for (int n = 0; n < 28; ++n) xs[n] = ld16(rs, lane * 16, o_xs + n * 1024);
			SB();
#pragma unroll
			for (int n = 14; n < 28; ++n) {
				W[n][0] = ld16(rs, lane * 16, o_w0 + ((n / 7) * 28 + n % 7) * 1024);
				W[n][1] = ld16(rs, lane * 16, o_w1 + ((n / 7) * 28 + n % 7) * 1024);
			}
			SB();
			f32x16 a0, a1, gr;
#pragma unroll
			for (int r = 0; r < 16; ++r) { a0[r] = 0.f; a1[r] = 0.f; gr[r] = 0.f; }
			u8* wp = sm + L_TOP + w * 8192 + lane * 16;
			const u8* tp = sm + L_TOP + w * 8192 + (8 * h + q4) * 64 + (g & 1) * 32 + (pc & 1) * 16 + (pc >> 1) * 8;
			const u8* ap = sm + lane * 16;
			u32x4 A[2][8];
			u32x2 tlo[4];
#pragma unroll
			for (int c = 0; c < 8; ++c) {
				if (c < 7) {
					unsigned m = 0;
#pragma unroll
					for (int k = 0; k < 4; ++k) m = mx4(m, xs[4 * c + k]);
					if (UNLIKELY(anyp(m))) {
						for (int it = 0; it < SPIN; ++it) {
							__builtin_amdgcn_s_sleep(2);
							FENCE();
							m = 0;
#pragma unroll
							for (int k = 0; k < 4; ++k) { xs[4 * c + k] = ld16(rs, lane * 16, o_xs + (4 * c + k) * 1024); m = mx4(m, xs[4 * c + k]); }
							if (!anyp(m)) break;
						}
					}
				}
				SB();
#pragma unroll
				for (int i = 0; i < 12; ++i) {
					if (c > 0) {
						const int k = i / 3, ks = 4 * (c - 1) + k;
						if (i % 3 == 0) a0 = mfma32(A[(c - 1) & 1][2 * k], B[ks], a0);
						else if (i % 3 == 1) a1 = mfma32(A[(c - 1) & 1][2 * k + 1], B[ks], a1);
						else {
							u32x4 bm = B[ks];
							if (ks == 0) bm[0] &= h ? 0xffffffffu : 0x0000ffffu;
							gr = mfma32(bm, B[ks], gr);
						}
					}
					if (c < 7) {
#pragma unroll
						for (int o = 2 * i; o < 2 * i + 2; ++o) {
							if (o < 4) *(u32x4*)(wp + ((4 * c + o) & 7) * 1024) = xs[4 * c + o];
							else if (o < 12) {
								const int k = (o - 4) >> 1, ks = 4 * c + k;
								if (o & 1) B[ks] = cat(tlo[k], trread(tp + (ks & 7) * 1024 + 256));
								else tlo[k] = trread(tp + (ks & 7) * 1024);
							} else if (o < 20) {
								const int k = (o - 12) >> 1;
								A[c & 1][o - 12] = *(const u32x4*)(ap + ((o & 1) * 28 + 4 * c + k) * 1024);
							}
						}
					}
					SB();
				}
			}
			if (l == 0) {
#pragma unroll
				for (int n = 7; n < 14; ++n) {
					W[n][0] = ld16(rs, lane * 16, o_w0 + (28 + n % 7) * 1024);
					W[n][1] = ld16(rs, lane * 16, o_w1 + (28 + n % 7) * 1024);
				}
			}
			float r2;
			{
				const int dr = 4 * ((lane & 31) >> 3) + (lane & 3);
				float ss = 0.f;
#pragma unroll
				for (int r = 0; r < 16; ++r) ss = (dr == r) ? gr[r] : ss;
				ss = (((lane >> 2) & 1) == h) ? ss : 0.f;
				int si = __builtin_bit_cast(int, ss);
				auto sw = __builtin_amdgcn_permlane32_swap(si, si, false, false);
				const unsigned s0 = sw[0], s1 = sw[1];
				ss = __builtin_bit_cast(float, s0) + __builtin_bit_cast(float, s1);
				float v = ss * (1.0f / 448.0f);
				float r1 = __builtin_amdgcn_rsqf(v + EPS);
				r2 = r1 * __builtin_amdgcn_rsqf(__builtin_fmaf(v * r1, r1, EPS));
			}
			const u8* tab = sm + L_TAB + h * 16;
			const unsigned o_hs = o_h + ((4 * pp + w) * 112 + 2 * ft) * 1024;
			f32x16 a2, a3;
#pragma unroll
			for (int r = 0; r < 16; ++r) { a2[r] = 0.f; a3[r] = 0.f; }
			SB();
#pragma unroll
			for (int half = 0; half < 2; ++half) {
#pragma unroll
				for (int ks = 14 * half; ks < 14 * half + 14; ++ks) {
					a2 = mfma32(*(const u32x4*)(ap + (56 + ks) * 1024), B[ks], a2);
					a3 = mfma32(*(const u32x4*)(ap + (84 + ks) * 1024), B[ks], a3);
				}
				gelu_store(half ? a1 : a0, r2, tab + half * 128, rs, lane * 16, o_hs + half * 28 * 1024);
				__builtin_amdgcn_sched_group_barrier(0x100, 6, 0);
#pragma unroll
				for (int i = 0; i < 28; ++i) {
					__builtin_amdgcn_sched_group_barrier(0x008, 1, 0);
					__builtin_amdgcn_sched_group_barrier(0x100, 1, 0);
					__builtin_amdgcn_sched_group_barrier(0x002, 5, 0);
				}
				SB();
			}
#pragma unroll
			for (int n = 0; n < 14; ++n) {
				H[n][0] = ld16(rs, lane * 16, o_h0 + ((n / 7) * 28 + n % 7) * 1024);
				H[n][1] = ld16(rs, lane * 16, o_h1 + ((n / 7) * 28 + n % 7) * 1024);
			}
			SB();
			gelu_store(a2, r2, tab + 256, rs, lane * 16, o_hs + 56 * 1024);
			SB();
			gelu_store(a3, r2, tab + 384, rs, lane * 16, o_hs + 84 * 1024);
			SB();
		}
		LBAR();
		{
			f32x16 acc[4];
#pragma unroll
			for (int i = 0; i < 4; ++i)
#pragma unroll
				for (int r = 0; r < 16; ++r) acc[i][r] = 0.f;
			u32x4 res[2], xq[2][7], qb[2][7];
			unsigned bias = 0;
#define H_RETRY(n0) \
			for (int it = 0; it < SPIN; ++it) { \
				__builtin_amdgcn_s_sleep(2); \
				FENCE(); \
				m = 0; \
				_Pragma("unroll") for (int k = (n0); k < (n0) + 2; ++k) { \
					H[k][0] = ld16(rs, lane * 16, o_h0 + ((k / 7) * 28 + k % 7) * 1024); \
					H[k][1] = ld16(rs, lane * 16, o_h1 + ((k / 7) * 28 + k % 7) * 1024); \
					m = mx4(mx4(m, H[k][0]), H[k][1]); \
				} \
				if (!anyp(m)) break; \
			}
			unsigned m = mx4(mx4(mx4(mx4(0, H[0][0]), H[0][1]), H[1][0]), H[1][1]);
			if (UNLIKELY(anyp(m))) { H_RETRY(0) }
			SB();
#pragma unroll
			for (int n = 0; n < 28; ++n) {
				const bool chk = !(n & 1) && n + 2 < 28;
				acc[0] = mfma32(H[n][0], W[n][0], acc[0]);
				if (chk) m = mx4(mx4(0, H[n + 2][0]), H[n + 2][1]);
				SB();
				acc[1] = mfma32(H[n][0], W[n][1], acc[1]);
				if (chk) m = mx4(mx4(m, H[n + 3][0]), H[n + 3][1]);
				SB();
				acc[2] = mfma32(H[n][1], W[n][0], acc[2]);
				SB();
				if (chk) { if (UNLIKELY(anyp(m))) { H_RETRY(n + 2) } }
				acc[3] = mfma32(H[n][1], W[n][1], acc[3]);
				if (n == N2 || n == N3) {
					const int s = (n == N2) ? 2 : 3;
#pragma unroll
					for (int k = 7 * s; k < 7 * s + 7; ++k) {
						H[k][0] = ld16(rs, lane * 16, o_h0 + (s * 28 + k % 7) * 1024);
						H[k][1] = ld16(rs, lane * 16, o_h1 + (s * 28 + k % 7) * 1024);
					}
				}
				if (n == 13) {
					bias = ld4(rs, (lane & 31) * 4, O_B2 + l * 1792 + (64 * jt + 32 * (w & 1)) * 4);
					if (l == 0) {
						const unsigned o = O_XT1 + (2 * pt + (w >> 1)) * STRIP + (64 * jt + 32 * (w & 1)) * 64;
						res[0] = ld16(rs, (lane & 31) * 64 + h * 16, o);
						res[1] = ld16(rs, (lane & 31) * 64 + h * 16 + 32, o);
					} else {
#pragma unroll
						for (int i = 0; i < 7; ++i) {
							xq[0][i] = ld16(rs, lane * 16, O_XT3 + (2 * pt + (w >> 1)) * STRIP + (7 * w + i) * 1024);
							xq[1][i] = ld16(rs, lane * 16, O_XT3 + (2 * pt + ((w >> 1) ^ 1)) * STRIP + (7 * w + i) * 1024);
						}
#pragma unroll
						for (int i = 0; i < 7; ++i) {
							qb[0][i] = ld16(rs, lane * 16, O_QT + ((2 * jt + (w & 1)) * 28 + 7 * w + i) * 1024);
							qb[1][i] = ld16(rs, lane * 16, O_QT + ((2 * jt + ((w & 1) ^ 1)) * 28 + 7 * w + i) * 1024);
						}
					}
				}
				SB();
			}
			u8* ex = sm + L_TOP;
			if (l == 1) {
				u8* wp = ex + w * 12288 + lane * 16;
				const u8* tp = ex + w * 12288 + (8 * h + q4) * 64 + (g & 1) * 32 + (pc & 1) * 16 + (pc >> 1) * 8;
#pragma unroll
				for (int a = 0; a < 2; ++a) {
#pragma unroll
					for (int i = 0; i < 7; ++i) *(u32x4*)(wp + ((7 * a + i) % 12) * 1024) = xq[a][i];
#pragma unroll
					for (int i = 0; i < 7; ++i) xq[a][i] = cat(trread(tp + ((7 * a + i) % 12) * 1024), trread(tp + ((7 * a + i) % 12) * 1024 + 256));
				}
#pragma unroll
				for (int a = 0; a < 2; ++a)
#pragma unroll
					for (int i = 0; i < 7; ++i) {
						acc[2 * a] = mfma32(xq[a][i], qb[0][i], acc[2 * a]);
						acc[2 * a + 1] = mfma32(xq[a][i], qb[1][i], acc[2 * a + 1]);
					}
				LBAR();
			}
#pragma unroll
			for (int i = 1; i < 4; ++i) {
				u8* p = ex + (w ^ i) * 12288 + (i - 1) * 4096 + lane * 16;
#pragma unroll
				for (int q = 0; q < 4; ++q) *(f32x4*)(p + q * 1024) = f32x4{acc[i][4 * q], acc[i][4 * q + 1], acc[i][4 * q + 2], acc[i][4 * q + 3]};
			}
			LBAR();
			float v[16];
			const float bf = __builtin_bit_cast(float, bias);
#pragma unroll
			for (int q = 0; q < 4; ++q) {
				const u8* p = ex + w * 12288 + q * 1024 + lane * 16;
				f32x4 s = (*(const f32x4*)p + *(const f32x4*)(p + 4096)) + *(const f32x4*)(p + 8192);
#pragma unroll
				for (int e = 0; e < 4; ++e) {
					v[4 * q + e] = (acc[0][4 * q + e] + bf) + s[e];
					if (l == 0) v[4 * q + e] += (e & 1) ? bfhi(res[q >> 1][2 * (q & 1) + (e >> 1)]) : bflo(res[q >> 1][2 * (q & 1) + (e >> 1)]);
				}
			}
			u32x4 X = {pack2c(v[0], v[1]), pack2c(v[2], v[3]), pack2c(v[4], v[5]), pack2c(v[6], v[7])};
			u32x4 Y = {pack2c(v[8], v[9]), pack2c(v[10], v[11]), pack2c(v[12], v[13]), pack2c(v[14], v[15])};
			if (l == 0) {
				swap16(X, Y);
				const unsigned o = O_XT2 + (2 * pt + (w >> 1)) * STRIP + (64 * jt + 32 * (w & 1)) * 64;
				const unsigned vo = i16 * 64 + (g & 1) * 32 + h * 16;
				st16(rs, X, vo, o);
				st16(rs, Y, vo + 1024, o);
				SB();
				const float keep = (64 * jt + 32 * (w & 1) + (lane & 31) == 1) ? 0.f : 1.f;
				float s[8];
#pragma unroll
				for (int i = 0; i < 8; ++i) {
					int a = __builtin_bit_cast(int, v[i] * v[i] * keep), c = __builtin_bit_cast(int, v[i + 8] * v[i + 8] * keep);
					auto r = __builtin_amdgcn_permlane16_swap(a, c, false, false);
					const unsigned r0 = r[0], r1 = r[1];
					s[i] = __builtin_bit_cast(float, r0) + __builtin_bit_cast(float, r1);
				}
#pragma unroll
				for (int i = 0; i < 4; ++i) s[i] = ((lane & 8) ? s[i + 4] : s[i]) + dpp<0x128>((lane & 8) ? s[i] : s[i + 4]);
#pragma unroll
				for (int i = 0; i < 2; ++i) s[i] = ((lane & 4) ? s[i + 2] : s[i]) + dpp<0x141>((lane & 4) ? s[i] : s[i + 2]);
				s[0] = ((lane & 2) ? s[1] : s[0]) + dpp<0x4e>((lane & 2) ? s[0] : s[1]);
				s[0] += dpp<0xb1>(s[0]);
				if (!(lane & 1)) *(float*)(ex + w * 12288 + 11264 + (16 * (g & 1) + 8 * h + ((lane >> 1) & 7)) * 4) = s[0];
				LBAR();
				if (tid < 8) {
					const u8* p = ex + (tid >> 2) * 24576 + 11264 + (tid & 3) * 32;
					f32x4 s0 = *(const f32x4*)p + *(const f32x4*)(p + 12288), s1 = *(const f32x4*)(p + 16) + *(const f32x4*)(p + 12304);
					st16(rs, u32x4{pack2c(s0[0], s0[1]), pack2c(s0[2], s0[3]), pack2c(s1[0], s1[1]), pack2c(s1[2], s1[3])}, tid * 16, O_SS1 + jt * 4096 + pt * 128);
				}
				SB();
				col_consts(K, w, lane);
			} else {
				u8* im = ex + w * 12288;
				*(u32x4*)(im + (lane & 31) * 64 + h * 16) = X;
				*(u32x4*)(im + (lane & 31) * 64 + h * 16 + 32) = Y;
#pragma unroll
				for (int u = 0; u < 2; ++u) {
					const u8* p = im + (8 * g + q4) * 64 + 32 * u + 8 * pc;
					*(u32x4*)(out + (32 * (32 * (w >> 1) + 16 * u + i16) + pt) * 896 + (64 * jt + 32 * (w & 1) + 8 * g) * 2) = cat(trread(p), trread(p + 256));
				}
			}
		}
	}
}

extern "C" void kernel_launch(void* const* d_in, const int* in_sizes, int n_in, void* d_out, int out_size, void* d_ws, size_t ws_size,
		hipStream_t stream) {
	k_all<<<256, 256, 0, stream>>>((const u8*)d_in[0], (const u8*)d_in[1], (const u8*)d_in[2], (const u8*)d_in[4], (const u8*)d_in[5],
		(const u8*)d_in[6], (const u16*)d_in[7], (u8*)d_out, (u8*)d_ws);
}
```

```cpp
#include <hip/hip_runtime.h>
#include <hip/hip_bf16.h>
#include <stdint.h>

typedef __bf16 bf16x8 __attribute__((ext_vector_type(8)));
typedef __bf16 bf16x2 __attribute__((ext_vector_type(2)));
typedef short s16x4 __attribute__((ext_vector_type(4)));
typedef float f32x2 __attribute__((ext_vector_type(2)));
typedef float f32x4 __attribute__((ext_vector_type(4)));
typedef float f32x16 __attribute__((ext_vector_type(16)));
typedef unsigned u32x2 __attribute__((ext_vector_type(2)));
typedef unsigned u32x4 __attribute__((ext_vector_type(4)));
typedef u32x4 u32x4u __attribute__((aligned(2)));
typedef unsigned char u8;
typedef unsigned short u16;
typedef __amdgpu_buffer_rsrc_t rsrc_t;
template <int V> struct IC { static constexpr int value = V; };
#define LAS __attribute__((address_space(3)))
#define DEV __device__ __forceinline__

constexpr unsigned STRIP = 28672u;
constexpr unsigned XT_B = 64u * STRIP;
constexpr unsigned FR_B = 1605632u;
constexpr unsigned H_B = 7340032u;
constexpr unsigned O_FLAGA = 0, O_FLAGB = 32768, O_FLAGP = 61440, O_EPOCH = 65536;
constexpr unsigned O_B2 = 66560;
constexpr unsigned O_SS0 = 70656, O_SS1 = 74752;
constexpr unsigned O_XT0 = 131072;
constexpr unsigned O_XT1 = O_XT0 + XT_B;
constexpr unsigned O_XT2 = O_XT1 + XT_B;
constexpr unsigned O_H1 = O_XT2 + XT_B;
constexpr unsigned O_XT3 = O_H1 + H_B;
constexpr unsigned O_H2 = O_XT3 + XT_B;
constexpr unsigned O_W1F = O_H2 + H_B;
constexpr unsigned O_W2F = O_W1F + FR_B;
constexpr unsigned O_W2O = O_W2F + FR_B;
constexpr unsigned O_QT = O_W2O + FR_B;
constexpr unsigned O_WT = O_QT + 401408;
constexpr unsigned WS_END = O_WT + XT_B;
constexpr unsigned L_TOP = 114688;
constexpr unsigned L_TAB = L_TOP + 48640;

constexpr float EPS = 1e-5f;
constexpr float SC1 = 0.04724555912615340f;
constexpr float SC2 = 0.06681531047810609f;
constexpr unsigned POIS = 0xffffffffu;
constexpr unsigned OOB = 0x80000000u;
#define SPINB 18
constexpr int SPIN = 1 << SPINB;

DEV unsigned pack2(float a, float b) {
	f32x2 v = {a, b};
	return __builtin_bit_cast(unsigned, __builtin_convertvector(v, bf16x2));
}
DEV unsigned pack2c(float a, float b) { return min(pack2(a, b), 0xfffffffeu); }
DEV float bflo(unsigned u) { return __builtin_bit_cast(float, u << 16); }
DEV float bfhi(unsigned u) { return __builtin_bit_cast(float, u & 0xffff0000u); }
DEV f32x4 mfma16(u32x4 a, u32x4 b, f32x4 c) {
	return __builtin_amdgcn_mfma_f32_16x16x32_bf16(__builtin_bit_cast(bf16x8, a), __builtin_bit_cast(bf16x8, b), c, 0, 0, 0);
}
DEV f32x16 mfma32(u32x4 a, u32x4 b, f32x16 c) {
	return __builtin_amdgcn_mfma_f32_32x32x16_bf16(__builtin_bit_cast(bf16x8, a), __builtin_bit_cast(bf16x8, b), c, 0, 0, 0);
}
DEV f32x4 mfma4(u32x2 a, u32x2 b, f32x4 c) {
	return __builtin_amdgcn_mfma_f32_4x4x4bf16_1k(__builtin_bit_cast(s16x4, a), __builtin_bit_cast(s16x4, b), c, 0, 0, 0);
}
DEV rsrc_t mkrs(const void* p, unsigned bytes) { return __builtin_amdgcn_make_buffer_rsrc((void*)p, 0, bytes, 0x00020000); }
DEV void dma16(rsrc_t rs, u8* lds, unsigned voff, unsigned soff) {
	__builtin_amdgcn_raw_ptr_buffer_load_lds(rs, (LAS void*)lds, 16, voff, soff, 0, 0);
}
DEV u32x4 ld16(rsrc_t rs, unsigned v, unsigned s) { return __builtin_amdgcn_raw_buffer_load_b128(rs, v, s, 16); }
DEV u32x2 ld8(rsrc_t rs, unsigned v, unsigned s) { return __builtin_amdgcn_raw_buffer_load_b64(rs, v, s, 16); }
DEV unsigned ld4(rsrc_t rs, unsigned v, unsigned s) { return __builtin_amdgcn_raw_buffer_load_b32(rs, v, s, 16); }
DEV void st16(rsrc_t rs, u32x4 d, unsigned v, unsigned s) { __builtin_amdgcn_raw_buffer_store_b128(d, rs, v, s, 16); }
DEV void st8(rsrc_t rs, u32x2 d, unsigned v, unsigned s) { __builtin_amdgcn_raw_buffer_store_b64(d, rs, v, s, 16); }
DEV void st4(rsrc_t rs, unsigned d, unsigned v, unsigned s) { __builtin_amdgcn_raw_buffer_store_b32(d, rs, v, s, 16); }
DEV u32x2 trread(const u8* lds) {
	return __builtin_bit_cast(u32x2, __builtin_amdgcn_ds_read_tr16_b64_v4i16((LAS s16x4*)lds));
}
DEV u32x4 cat(u32x2 a, u32x2 b) { return u32x4{a[0], a[1], b[0], b[1]}; }
DEV float cosr(float x) { return __builtin_amdgcn_cosf(x); }
DEV float sinr(float x) { return __builtin_amdgcn_sinf(x); }
DEV unsigned mx4(unsigned m, u32x4 v) { return max(max(max(m, v[0]), v[1]), max(max(v[2], v[3]), 0u)); }
DEV bool anyp(unsigned m) { return __builtin_amdgcn_ballot_w64(m == POIS) != 0; }
DEV void swap32(u32x4& a, u32x4& b) {
#pragma unroll
	for (int i = 0; i < 4; ++i) {
		auto r = __builtin_amdgcn_permlane32_swap(a[i], b[i], false, false);
		a[i] = r[0]; b[i] = r[1];
	}
}
DEV void swap16(u32x4& a, u32x4& b) {
#pragma unroll
	for (int i = 0; i < 4; ++i) {
		auto r = __builtin_amdgcn_permlane16_swap(a[i], b[i], false, false);
		a[i] = r[0]; b[i] = r[1];
	}
}
#define WAIT_BAR(N) asm volatile("s_waitcnt vmcnt(" #N ") lgkmcnt(0)\n\ts_barrier" ::: "memory")
#define LBAR() asm volatile("s_waitcnt lgkmcnt(0)\n\ts_barrier" ::: "memory")
#define FENCE() asm volatile("" ::: "memory")
#define PIN(x) asm volatile("" : "+v"(x))
#define PINA(x) asm volatile("" : "+a"(x))
#define WAITV(N) asm volatile("s_waitcnt vmcnt(" #N ")" ::: "memory")
#define SB() __builtin_amdgcn_sched_barrier(0)
#define UNLIKELY(x) __builtin_expect(!!(x), 0)

DEV void poll_flag(rsrc_t rs, bool act, unsigned v, unsigned s, u32x2 tag) {
	bool ok = !act;
#pragma unroll 1
	for (int it = 0; it < SPIN; ++it) {
		if (!ok) {
			u32x2 a = ld8(rs, v, s);
			ok = (a[0] == tag[0]) & (a[1] == tag[1]);
		}
		if (__builtin_amdgcn_ballot_w64(!ok) == 0) break;
		__builtin_amdgcn_s_sleep(2);
		FENCE();
	}
	FENCE();
}

DEV void basis(int j, float& kq, float& ph) {
	int c = j >> 1;
	int kk = c ? c : ((j & 1) ? 0 : 224);
	kq = (float)kk * (1.0f / 448.0f);
	ph = (c && (j & 1)) ? 0.25f : 0.0f;
}

struct ColK {
	u32x4 C1[2], S1[2], C2[2], S2[2];
	float twc[8], tws[8], tic[8], tis[8];
};
DEV void col_part(ColK& K, int part, int w, int g, int i16) {
	const float f1 = (float)(16 * w + i16);
	if (part < 8) {
		const int ks = part >> 2, i = part & 3;
		float a = f1 * (float)(32 * ks + 8 * g + 2 * i) * (1.0f / 64.0f), bb = a + f1 * (1.0f / 64.0f);
		unsigned c = pack2(cosr(a), cosr(bb)), s = pack2(sinr(a), sinr(bb));
		PIN(c); PIN(s);
		K.C1[ks][i] = c; K.S1[ks][i] = s;
	} else if (part < 16) {
		const int t2 = (part >> 2) & 1, i = part & 3;
		float m = (float)(16 * t2 + i16) * (1.0f / 32.0f);
		float a = m * (float)(16 * (i >> 1) + 4 * g + 2 * (i & 1)), bb = a + m;
		unsigned c = pack2(cosr(a), cosr(bb)), s = pack2(sinr(a), sinr(bb));
		PIN(c); PIN(s);
		K.C2[t2][i] = c; K.S2[t2][i] = s;
	} else {
		const int i = part & 7;
		float a = f1 * (float)(16 * (i >> 2) + 4 * g + (i & 3)) * (1.0f / 2048.0f);
		K.twc[i] = cosr(a);
		K.tws[i] = sinr(a);
		float bb = (float)(16 * w + 4 * g + (i & 3)) * (float)(16 * (i >> 2) + i16) * (1.0f / 2048.0f);
		K.tic[i] = cosr(bb) * (1.0f / 2048.0f);
		K.tis[i] = sinr(bb) * (1.0f / 2048.0f);
		PIN(K.twc[i]); PIN(K.tws[i]); PIN(K.tic[i]); PIN(K.tis[i]);
	}
}

struct GS {
	float pre[2], t[2];
	unsigned o[8];
	f32x4 bb[4];
};
DEV void gelu_blk(GS& s, int k, const f32x16& acc, float r) {
	const int p = k / 3, j = k % 3;
	if (j == 0) {
		if (p) {
			s.o[p - 1] = pack2c(s.t[0], s.t[1]);
			PIN(s.o[p - 1]);
		}
		if (p < 8) {
			s.pre[0] = __builtin_fmaf(acc[2 * p], r, s.bb[p >> 1][(2 * p) & 3]);
			s.pre[1] = __builtin_fmaf(acc[2 * p + 1], r, s.bb[p >> 1][(2 * p + 1) & 3]);
			s.t[0] = __builtin_fmaf(s.pre[0] * s.pre[0], -0.10294324f, -2.30220820f);
			s.t[1] = s.pre[1] * s.pre[1];
			asm volatile("" : "+v"(s.pre[0]), "+v"(s.pre[1]), "+v"(s.t[0]), "+v"(s.t[1]));
		}
	} else if (j == 1) {
		s.t[1] = __builtin_fmaf(s.t[1], -0.10294324f, -2.30220820f);
		s.t[0] = __builtin_amdgcn_exp2f(s.pre[0] * s.t[0]);
		s.t[1] = __builtin_amdgcn_exp2f(s.pre[1] * s.t[1]);
		asm volatile("" : "+v"(s.t[0]), "+v"(s.t[1]));
	} else {
		s.t[0] = s.pre[0] * __builtin_amdgcn_rcpf(1.0f + s.t[0]);
		s.t[1] = s.pre[1] * __builtin_amdgcn_rcpf(1.0f + s.t[1]);
		asm volatile("" : "+v"(s.t[0]), "+v"(s.t[1]));
	}
}
template <int CTRL> DEV float dpp(float x) {
	return __builtin_bit_cast(float, __builtin_amdgcn_update_dpp(0, __builtin_bit_cast(int, x), CTRL, 0xf, 0xf, true));
}

#define VA 26
#define HS0 16

DEV void check_flag(rsrc_t rs, u32x2 a, bool act, unsigned v, unsigned s, u32x2 tag) {
	const bool bad = act & ((a[0] != tag[0]) | (a[1] != tag[1]));
	if (UNLIKELY(__builtin_amdgcn_ballot_w64(bad) != 0)) poll_flag(rs, bad, v, s, tag);
	FENCE();
}
template <int HI> DEV void p0_gemm(const u8* sm, const u32x4 (&qf)[14], f32x4 (&acc)[4], int g, int i16) {
	unsigned sg = 2 * (i16 >> 2) + ((i16 >> 1) & 1);
	const u8* base = sm + (8 * (i16 >> 2) + (i16 & 3)) * 896;
	const u8* bp[4] = {base + ((g ^ sg) * 16), base + (((4 + g) ^ sg) * 16), base + ((g ^ sg) * 16) + HI, base + (((4 + g) ^ sg) * 16) + HI};
	u32x4 a[8];
#pragma unroll
	for (int i = -8; i < 56; ++i) {
		if (i >= 0) {
			acc[i & 3] = mfma16(a[i & 7], qf[i >> 2], i < 4 ? f32x4{0.f, 0.f, 0.f, 0.f} : acc[i & 3]);
			PINA(acc[i & 3]);
		}
		const int n = i + 8, ks = n >> 2, t = n & 3;
		if (n < 56) a[n & 7] = *(const u32x4*)(bp[(ks & 1) + (t & 2)] + (ks >> 1) * 128 + (t & 1) * 3584);
		SB();
	}
}
DEV float dot2(unsigned a, unsigned b, float c) {
	return __builtin_amdgcn_fdot2_f32_bf16(__builtin_bit_cast(bf16x2, a), __builtin_bit_cast(bf16x2, b), c, false);
}
__global__ __launch_bounds__(256, 1) void k_all(const u8* __restrict__ x, const u8* __restrict__ wr, const u8* __restrict__ wi,
		const u8* __restrict__ w1, const u8* __restrict__ b1, const u8* __restrict__ w2, const u8* __restrict__ b2,
		u8* out, u8* ws) {
	__shared__ __attribute__((aligned(16))) u8 sm[163840];
	const int tid = threadIdx.x, lane = tid & 63;
	int w = __builtin_amdgcn_readfirstlane(tid >> 6);
	asm volatile("" : "+s"(w));
	const int g = lane >> 4, i16 = lane & 15, h = lane >> 5;
	const int q4 = (lane >> 2) & 3, pc = lane & 3;
	const int b = blockIdx.x;
	const rsrc_t rs = mkrs(ws, WS_END);
	const u32x4 pois = {POIS, POIS, POIS, POIS};
	const u32x2 ep = ld8(rs, 0, O_EPOCH);
	u32x2 tag;
	if (b >= 224) {
		const int sb = b - 224;
		const int li = lane & 31;
		u32x4 v[16];
		if (li < 29) {
			const u8* src = ((w >> 1) ? wi : wr) + (2 * sb + (w & 1) + 64 * h) * 450 + (li < 28 ? 16 * li : 434);
#pragma unroll
			for (int n = 0; n < 16; ++n) v[n] = *(const u32x4u*)(src + n * (128 * 450));
		}
		SB();
		const unsigned long long tg = (((unsigned long long)ep[1] << 32) | ep[0]) + 0x9e3779b97f4a7c15ull;
		tag = u32x2{(unsigned)tg, (unsigned)(tg >> 32)};
		st8(rs, tag, tid ? OOB : 0u, O_FLAGA + b * 128);
		SB();
		if (li < 29) {
			u8* dst = sm + (32 * w + h) * 464;
#pragma unroll
			for (int n = 0; n < 16; ++n) {
				*(u32x4*)(dst + n * 928 + li * 16) = v[n];
				if (li == 28) *(u16*)(dst + n * 928) = (u16)(v[n][3] >> 16);
			}
		}
		poll_flag(rs, tid < 224, tid * 128, O_FLAGA, tag);
		LBAR();
#pragma unroll
		for (int n = 0; n < 7; ++n) {
			const int task = w + 4 * n, cgp = task >> 1, fl = task & 1;
#pragma unroll
			for (int t = 0; t < 2; ++t) {
				const u8* p = sm + (fl * 32 + 16 * t + 4 * g + q4) * 464 + cgp * 32 + pc * 8;
				u32x2 re = trread(p), im = trread(p + 64 * 464);
				u32x4 o = {min(__builtin_amdgcn_perm(im[0], re[0], 0x05040100u), 0xfffffffeu), min(__builtin_amdgcn_perm(im[0], re[0], 0x07060302u), 0xfffffffeu),
						   min(__builtin_amdgcn_perm(im[1], re[1], 0x05040100u), 0xfffffffeu), min(__builtin_amdgcn_perm(im[1], re[1], 0x07060302u), 0xfffffffeu)};
				st16(rs, o, i16 * 8192 + g * 16 + t * 64, O_WT + cgp * 131072 + (2 * sb + fl) * 128);
			}
		}
#pragma unroll 1
		for (int fr = sb * 4 + w; fr < 392; fr += 128) {
			int dt = fr / 28, ks = fr % 28;
			float d = (float)(32 * dt + li);
			float c[8];
#pragma unroll
			for (int i = 0; i < 8; ++i) {
				int j = 16 * ks + 8 * h + i;
				float kq, ph;
				basis(j, kq, ph);
				c[i] = cosr(__builtin_fmaf(kq, d, ph)) * ((j < 2) ? SC1 : SC2);
			}
			st16(rs, u32x4{pack2(c[0], c[1]), pack2(c[2], c[3]), pack2(c[4], c[5]), pack2(c[6], c[7])}, lane * 16, O_QT + fr * 1024);
		}
#pragma unroll 2
		for (int n = 0; n < 70; ++n) st16(rs, pois, tid * 16, O_XT3 + (sb * 70 + n) * 4096);
		WAIT_BAR(0);
		if (tid == 0) st8(rs, tag, 0, O_FLAGP + sb * 128);
		return;
	}
	const int xcd = b & 7, slot = b >> 3, s7 = slot / 7, jt = slot % 7;
	const int pp = 4 * (xcd >> 1) + s7, ft = 7 * (xcd & 1) + jt, pt = 2 * pp + (xcd & 1);
	ColK K;
	u32x2 b1r;
	{
		const int p0 = 4 * xcd + s7;
		const bool fold = p0 < 28;
		const int pf = fold ? p0 : p0 - 28;
		u8* bufW = sm + 57344;
		const unsigned o_xt0 = O_XT0 + 2 * p0 * STRIP + (64 * jt + 16 * w) * 64, v_xt0 = i16 * 64 + g * 16;
		const unsigned o_ss0 = O_SS0 + (64 * p0 + 16 * jt + 4 * w) * 2, v_ss0 = (lane == 0 && jt < 4) ? 0u : OOB;
		const unsigned w1f_o = O_W1F + (2 * pf * 28 + 4 * jt + w) * 1024, v_f = fold ? lane * 16 : OOB;
		st16(rs, pois, v_xt0, o_xt0);
		st16(rs, pois, v_xt0, o_xt0 + STRIP);
		st8(rs, u32x2{POIS, POIS}, v_ss0, o_ss0);
		st16(rs, pois, tid * 16, O_WT + b * 8192);
		st16(rs, pois, tid * 16, O_WT + b * 8192 + 4096);
		st16(rs, pois, v_f, w1f_o);
		st16(rs, pois, v_f, w1f_o + 28 * 1024);
		SB();
		const rsrc_t rx = mkrs(x, 2048u * 896u), r1 = mkrs(w1, 448u * 3584u), r2 = mkrs(w2, 1792u * 896u);
		const int vw[2] = {(lane >> 3) * 3584 + (((lane & 7) ^ (((lane >> 4) & 1) << 1)) * 16), (lane >> 3) * 3584 + (((lane & 7) ^ ((((lane >> 4) & 1) | 2) << 1)) * 16)};
		unsigned vx[7], v2[7];
		u32x4 w2r[7];
		const int j = 64 * jt + 16 * w + i16;
		float kq, ph;
		basis(j, kq, ph);
		const float a0 = (float)(8 * g) * kq + ph;
		u32x4 qf[14];
#pragma unroll
		for (int v = 0; v < 42; ++v) {
			if (v < 14) {
				const int n = v % 7;
				if (v < 7) {
					const int up = (8 * n + lane >= 56), dr = n + up;
					const unsigned c = ((unsigned)(8 * n + lane - 56 * up) ^ (unsigned)(2 * w + ((dr >> 1) & 1))) * 16;
					vx[n] = dr * 28672 + c;
					v2[n] = dr * 896 + c;
				}
				const int r = 32 * (v / 7) + 8 * w;
				dma16(rx, sm + r * 896 + n * 1024, vx[n], p0 * 896u + r * 28672u);
			} else if (v < 28) {
				const int i = 14 * w + v - 14;
				dma16(r1, bufW + i * 1024, vw[v & 1], i * (8 * 3584) + pf * 128);
			} else if (v < 35) {
				dma16(r2, sm + L_TOP + 8 * w * 896 + (v - 28) * 1024, v2[v - 28], (64 * pf + 16 * w) * 896u);
			} else {
				w2r[v - 35] = __builtin_amdgcn_raw_buffer_load_b128(r2, lane * 16, (64 * pf + 16 * w + 8) * 896u + (v - 35) * 1024, 0);
				if (v == 41) dma16(mkrs(b2, 896u), sm + L_TOP + 28672, lane * 16, 0);
			}
			SB();
			if (v < 28) {
#pragma unroll
				for (int i = 2 * (v & 1); i < 2 * (v & 1) + 2; ++i) {
					const int ks = v >> 1;
					unsigned c = pack2(cosr(__builtin_fmaf(kq, (float)(32 * ks + 2 * i), a0)), cosr(__builtin_fmaf(kq, (float)(32 * ks + 2 * i + 1), a0)));
					PIN(c);
					qf[ks][i] = c;
				}
			} else {
#pragma unroll
				for (int part = (v - 28) * 12 / 7; part < (v - 27) * 12 / 7; ++part) col_part(K, part, w, g, i16);
			}
			if (v == VA) {
				u32x2 e2 = ep;
				PIN(e2);
				const unsigned long long tg = (((unsigned long long)e2[1] << 32) | e2[0]) + 0x9e3779b97f4a7c15ull;
				tag = u32x2{(unsigned)tg, (unsigned)(tg >> 32)};
				asm volatile("s_waitcnt vmcnt(%0)\n\ts_barrier" ::"n"(VA + 1) : "memory");
				st8(rs, tag, tid ? OOB : 0u, O_FLAGA + b * 128);
			}
			SB();
		}
		b1r = *(const u32x2*)(b1 + (128 * ft + 4 * (lane & 31)) * 2);
		const u32x2 fa = ld8(rs, tid * 128, O_FLAGA);
		const float scj = (j < 2) ? SC1 : SC2;
		WAIT_BAR(32);
		f32x4 acc[4];
		p0_gemm<28672>(sm, qf, acc, g, i16);
#pragma unroll
		for (int u = 0; u < 2; ++u) {
			u32x4 o = {pack2c(acc[2 * u][0] * scj, acc[2 * u][1] * scj), pack2c(acc[2 * u][2] * scj, acc[2 * u][3] * scj),
					   pack2c(acc[2 * u + 1][0] * scj, acc[2 * u + 1][1] * scj), pack2c(acc[2 * u + 1][2] * scj, acc[2 * u + 1][3] * scj)};
			st16(rs, o, v_xt0, o_xt0 + u * STRIP);
		}
		SB();
		{
			const u8* rb = sm + (16 * (jt & 3) + 4 * w + g) * 896 + i16 * 16;
			float s1 = 0.f, s2 = 0.f;
#pragma unroll
			for (int q = 0; q < 4; ++q) {
				u32x4 v = *(const u32x4*)(rb + q * 256);
#pragma unroll
				for (int i = 0; i < 4; ++i) {
					unsigned e = v[i];
					if (q == 3) e = (i16 < 8) ? e : 0u;
					s1 = dot2(e, 0x3f803f80u, s1);
					s2 = dot2(e, e, s2);
				}
			}
			s1 += dpp<0x128>(s1); s2 += dpp<0x128>(s2);
			s1 += dpp<0x124>(s1); s2 += dpp<0x124>(s2);
			s1 += dpp<0x122>(s1); s2 += dpp<0x122>(s2);
			s1 += dpp<0x121>(s1); s2 += dpp<0x121>(s2);
			int sv = __builtin_bit_cast(int, fmaxf(s2 - s1 * s1 * (1.0f / 448.0f), 0.f));
			float t0 = __builtin_bit_cast(float, __builtin_amdgcn_readlane(sv, 0)), t1 = __builtin_bit_cast(float, __builtin_amdgcn_readlane(sv, 16));
			float t2 = __builtin_bit_cast(float, __builtin_amdgcn_readlane(sv, 32)), t3 = __builtin_bit_cast(float, __builtin_amdgcn_readlane(sv, 48));
			st8(rs, u32x2{pack2c(t0, t1), pack2c(t2, t3)}, v_ss0, o_ss0);
		}
		WAIT_BAR(3);
#pragma unroll
		for (int n = 0; n < 7; ++n) *(u32x4*)(sm + 8 * w * 896 + v2[n]) = w2r[n];
		SB();
		{
			const unsigned kap = ((q4 >> 1) & 1) | ((g & 1) << 1);
			const u8* rb = bufW + (8 * g + q4) * 128 + (pc & 1) * 8;
			const u8* bp[4];
#pragma unroll
			for (int t = 0; t < 4; ++t) bp[t] = rb + (((2 * t + (pc >> 1)) ^ (kap << 1)) * 16);
			const unsigned xo = (16 * w + ((lane & 31) >> 2)) * STRIP + h * 64 + pc * 16;
			u32x2 lo[8], hi[8];
#pragma unroll
			for (int i = -8; i < 56; ++i) {
				if (i >= 0) {
					acc[i & 3] = mfma16(qf[i >> 2], cat(lo[i & 7], hi[i & 7]), i < 4 ? f32x4{0.f, 0.f, 0.f, 0.f} : acc[i & 3]);
					PINA(acc[i & 3]);
				}
				const int n = i + 8;
				if (n < 56) {
					lo[n & 7] = trread(bp[n & 3] + (n >> 2) * 4096);
					hi[n & 7] = trread(bp[n & 3] + (n >> 2) * 4096 + 512);
				}
				if (i >= 0 && (i & 3) == 0) {
					const int k = i >> 2;
					if (k < 10) st16(rs, pois, tid * 16, O_XT2 + (b * 10 + k) * 4096);
					else if (k < 12) st16(rs, pois, xo, O_XT1 + b * 128 + (k - 10) * 8 * STRIP);
					else if (k == 12) st16(rs, pois, tid < 8 ? tid * 16 : OOB, O_SS1 + b * 128);
				}
				SB();
			}
			const int j0 = 64 * jt + 16 * w + 4 * g;
			const float s0 = (j0 == 0) ? SC1 : SC2, s1 = (j0 == 0) ? 0.f : SC2;
#pragma unroll
			for (int u = 0; u < 2; ++u) {
				unsigned x0 = pack2c(acc[2 * u][0] * s0, acc[2 * u][1] * s1), x1 = pack2c(acc[2 * u][2] * SC2, acc[2 * u][3] * SC2);
				unsigned y0 = pack2c(acc[2 * u + 1][0] * s0, acc[2 * u + 1][1] * s1), y1 = pack2c(acc[2 * u + 1][2] * SC2, acc[2 * u + 1][3] * SC2);
				auto r0 = __builtin_amdgcn_permlane16_swap(x0, y0, false, false);
				auto r1v = __builtin_amdgcn_permlane16_swap(x1, y1, false, false);
				st16(rs, u32x4{r0[0], r1v[0], r0[1], r1v[1]}, v_f, w1f_o + u * 28 * 1024);
			}
		}
		check_flag(rs, fa, true, tid * 128, O_FLAGA, tag);
		LBAR();
		st8(rs, tag, (b | tid) ? OOB : 0u, O_EPOCH);
		{
			p0_gemm<-(int)L_TOP>(sm + L_TOP, qf, acc, g, i16);
			const int kk = 4 * pf + g;
			const unsigned kv = fold ? (28 * ((kk >> 1) & 3) + 2 * (kk >> 3) + (kk & 1)) * 1024 + (16 * (w & 1) + i16) * 16 : OOB;
#pragma unroll
			for (int hh = 0; hh < 2; ++hh) {
				u32x4 o = {pack2(acc[hh][0] * scj, acc[hh][1] * scj), pack2(acc[hh][2] * scj, acc[hh][3] * scj),
						   pack2(acc[2 + hh][0] * scj, acc[2 + hh][1] * scj), pack2(acc[2 + hh][2] * scj, acc[2 + hh][3] * scj)};
				st16(rs, o, kv + hh * 512, O_W2F + (2 * jt + (w >> 1)) * 112 * 1024);
			}
			const int kw = 4 * pf + w;
			const unsigned ko = (28 * ((kw >> 1) & 3) + 2 * (kw >> 3) + (kw & 1)) * 1024;
			const unsigned sg = 2 * w + ((q4 >> 1) & 1);
#pragma unroll
			for (int u = 0; u < 2; ++u) {
				unsigned ch = 8 * jt + ((4 * u + 2 * (g & 1) + (pc >> 1)) ^ sg);
				const u8* p = sm + (8 * w + 4 * h + q4) * 896 + ch * 16 + (pc & 1) * 8;
				st16(rs, cat(trread(p + L_TOP), trread(p)), v_f, O_W2O + (2 * jt + u) * 112 * 1024 + ko);
			}
			if (p0 == 28) {
				f32x4 c = {0.f, 0.f, 0.f, 0.f};
#pragma unroll
				for (int ks = 0; ks < 14; ++ks) c = mfma16(*(const u32x4*)(sm + L_TOP + 28672 + 64 * ks + 16 * g), qf[ks], c);
				const float c0 = c[0];
				st4(rs, __builtin_bit_cast(unsigned, c0 * scj), g ? OOB : i16 * 4, O_B2 + (64 * jt + 16 * w) * 4);
			}
		}
	}
	u32x4 wv[2];
	auto layer = [&](auto LC) __attribute__((always_inline)) {
		constexpr int l = decltype(LC)::value;
		int tid = threadIdx.x;
		PIN(tid);
		const int lane = tid & 63, g = lane >> 4, i16 = lane & 15, h = lane >> 5, q4 = (lane >> 2) & 3, pc = lane & 3;
		const unsigned o_ci = l ? O_XT2 : O_XT0, o_co = l ? O_XT3 : O_XT1, o_h = l ? O_H2 : O_H1;
		const unsigned o_w[2] = {(l ? O_W2O : O_W2F) + ((2 * jt + (w & 1)) * 112 + 7 * w) * 1024, (l ? O_W2O : O_W2F) + ((2 * jt + ((w & 1) ^ 1)) * 112 + 7 * w) * 1024};
		const unsigned o_hl[2] = {o_h + ((2 * pt + (w >> 1)) * 112 + 7 * w) * 1024, o_h + ((2 * pt + ((w >> 1) ^ 1)) * 112 + 7 * w) * 1024};
		const unsigned l16 = lane * 16;
		u32x4 W[28][2], H[28][2];
		u32x4 w1s[28];
		u32x2 fb[4];
#define LDF(arr, base, q) arr[(q) >> 1][(q) & 1] = ld16(rs, l16 + ((((q) >> 1) % 7) & 3) * 1024, base[(q) & 1] + ((((q) >> 1) / 7) * 28 + ((((q) >> 1) % 7) & 4)) * 1024)
#define LDW(q) LDF(W, o_w, q)
#define LDH(q) LDF(H, o_hl, q)
#define LDW1(n) w1s[n] = ld16(rs, l16 + ((n) & 3) * 1024, O_W1F + ((4 * ft + w) * 28 + ((n) & ~3)) * 1024)
#define HOOK(k) do { SB(); if (l == 0) { LDW1(2 * (k)); LDW1(2 * (k) + 1); } else { LDW(2 * (k)); LDW(2 * (k) + 1); } SB(); } while (0)
		{
			const unsigned xo = (16 * w + ((lane & 31) >> 2)) * STRIP + h * 64 + pc * 16;
			const unsigned wo = (16 * w + i16) * 128 + g * 16;
			u32x4 xr, xi, ssp[7];
			unsigned m;
#define COL_LOAD() do { \
				xr = ld16(rs, xo, o_ci + b * 128); xi = ld16(rs, xo, o_ci + b * 128 + 8 * STRIP); \
				if (l == 0) { \
					ssp[0] = ld16(rs, tid * 16, O_SS0); \
					wv[0] = ld16(rs, wo, O_WT + b * 8192); wv[1] = ld16(rs, wo + 64, O_WT + b * 8192); \
				} else { \
					_Pragma("unroll") for (int k = 0; k < 7; ++k) ssp[k] = ld16(rs, tid * 16, O_SS1 + k * 4096); \
				} } while (0)
#define COL_MAX() do { \
				m = mx4(mx4(0, xr), xi); \
				if (l == 0) m = mx4(mx4(mx4(m, ssp[0]), wv[0]), wv[1]); \
				else { _Pragma("unroll") for (int k = 0; k < 7; ++k) m = mx4(m, ssp[k]); } } while (0)
			COL_LOAD();
			if (l == 0) LBAR();
			COL_MAX();
			if (UNLIKELY(anyp(m))) {
#pragma unroll 1
				for (int it = 0; it < SPIN; ++it) {
					__builtin_amdgcn_s_sleep(2);
					FENCE();
					if (m == POIS) { COL_LOAD(); COL_MAX(); }
					if (!anyp(m)) break;
				}
			}
			swap32(xr, xi);
			float fr[8], fi[8];
			u8* cs = sm + L_TOP;
			{
				float rr[8];
#pragma unroll
				for (int i = 0; i < 4; ++i) {
					float lo = bflo(ssp[0][i]), hi = bfhi(ssp[0][i]);
					if (l == 1) {
#pragma unroll
						for (int k = 1; k < 7; ++k) { lo = dot2(ssp[k][i], 0x00003f80u, lo); hi = dot2(ssp[k][i], 0x3f800000u, hi); }
					}
					rr[2 * i] = __builtin_amdgcn_rsqf(__builtin_fmaf(lo, 1.0f / 448.0f, EPS));
					rr[2 * i + 1] = __builtin_amdgcn_rsqf(__builtin_fmaf(hi, 1.0f / 448.0f, EPS));
					fr[2 * i] = bflo(xr[i]); fr[2 * i + 1] = bfhi(xr[i]);
					fi[2 * i] = bflo(xi[i]); fi[2 * i + 1] = bfhi(xi[i]);
				}
				const unsigned imask = b ? 0xffffffffu : 0u;
				u8* p = cs + (tid >> 3) * 160 + (tid & 7) * 16;
				*(u32x4*)p = u32x4{pack2(fr[0] * rr[0], fr[1] * rr[1]), pack2(fr[2] * rr[2], fr[3] * rr[3]), pack2(fr[4] * rr[4], fr[5] * rr[5]), pack2(fr[6] * rr[6], fr[7] * rr[7])};
				*(u32x4*)(p + 5120) = u32x4{pack2(fi[0] * rr[0], fi[1] * rr[1]) & imask, pack2(fi[2] * rr[2], fi[3] * rr[3]) & imask,
											pack2(fi[4] * rr[4], fi[5] * rr[5]) & imask, pack2(fi[6] * rr[6], fi[7] * rr[7]) & imask};
			}
			HOOK(0);
			WAIT_BAR(2);
			if (l == 0) st8(rs, tag, tid ? OOB : 0u, O_FLAGB + b * 128);
			const f32x4 z4 = {0.f, 0.f, 0.f, 0.f};
			const u32x4 sgn = {0x80008000u, 0x80008000u, 0x80008000u, 0x80008000u};
			const u32x4 N1[2] = {K.S1[0] ^ sgn, K.S1[1] ^ sgn}, N2v[2] = {K.S2[0] ^ sgn, K.S2[1] ^ sgn};
			f32x4 are[2], aim[2];
			u32x4 Bre, Bim;
			u32x4 mr[4], mi[4];
#pragma unroll
			for (int q = 0; q < 4; ++q) {
				const u8* p = cs + (16 * (q >> 1) + i16) * 160 + 64 * (q & 1) + 16 * g;
				mr[q] = *(const u32x4*)p; mi[q] = *(const u32x4*)(p + 5120);
			}
			HOOK(1);
#pragma unroll
			for (int ks = 0; ks < 2; ++ks) {
#pragma unroll
				for (int t = 0; t < 2; ++t) {
					are[t] = mfma16(mr[2 * t + ks], K.C1[ks], ks ? are[t] : z4);
					aim[t] = mfma16(mi[2 * t + ks], K.C1[ks], ks ? aim[t] : z4);
				}
#pragma unroll
				for (int t = 0; t < 2; ++t) {
					are[t] = mfma16(mi[2 * t + ks], K.S1[ks], are[t]);
					aim[t] = mfma16(mr[2 * t + ks], N1[ks], aim[t]);
				}
				PINA(are[0]); PINA(are[1]); PINA(aim[0]); PINA(aim[1]);
				HOOK(2 + ks);
			}
#pragma unroll
			for (int t = 0; t < 2; ++t)
#pragma unroll
				for (int e = 0; e < 4; e += 2) {
					int i = 4 * t + e;
					Bre[i >> 1] = pack2(are[t][e] * K.twc[i] + aim[t][e] * K.tws[i], are[t][e + 1] * K.twc[i + 1] + aim[t][e + 1] * K.tws[i + 1]);
					Bim[i >> 1] = pack2(aim[t][e] * K.twc[i] - are[t][e] * K.tws[i], aim[t][e + 1] * K.twc[i + 1] - are[t][e + 1] * K.tws[i + 1]);
				}
			PIN(Bre); PIN(Bim);
			HOOK(4);
#pragma unroll
			for (int t = 0; t < 2; ++t) {
				are[t] = mfma16(K.C2[t], Bre, z4);
				aim[t] = mfma16(K.C2[t], Bim, z4);
			}
#pragma unroll
			for (int t = 0; t < 2; ++t) {
				are[t] = mfma16(K.S2[t], Bim, are[t]);
				aim[t] = mfma16(N2v[t], Bre, aim[t]);
			}
			PINA(are[0]); PINA(are[1]); PINA(aim[0]); PINA(aim[1]);
			HOOK(5);
#pragma unroll
			for (int t = 0; t < 2; ++t)
#pragma unroll
				for (int e = 0; e < 4; e += 2) {
					float a0 = bflo(wv[t][e]), b0 = bfhi(wv[t][e]), a1 = bflo(wv[t][e + 1]), b1v = bfhi(wv[t][e + 1]);
					Bre[2 * t + (e >> 1)] = pack2(are[t][e] * a0 - aim[t][e] * b0, are[t][e + 1] * a1 - aim[t][e + 1] * b1v);
					Bim[2 * t + (e >> 1)] = pack2(are[t][e] * b0 + aim[t][e] * a0, are[t][e + 1] * b1v + aim[t][e + 1] * a1);
				}
			PIN(Bre); PIN(Bim);
			HOOK(6);
#pragma unroll
			for (int t = 0; t < 2; ++t) {
				are[t] = mfma16(Bre, K.C2[t], z4);
				aim[t] = mfma16(Bim, K.C2[t], z4);
			}
#pragma unroll
			for (int t = 0; t < 2; ++t) {
				are[t] = mfma16(Bim, N2v[t], are[t]);
				aim[t] = mfma16(Bre, K.S2[t], aim[t]);
			}
			PINA(are[0]); PINA(are[1]); PINA(aim[0]); PINA(aim[1]);
			HOOK(7);
#pragma unroll
			for (int t = 0; t < 2; ++t) {
				float vr[4], vi[4];
#pragma unroll
				for (int e = 0; e < 4; ++e) {
					vr[e] = are[t][e] * K.tic[4 * t + e] - aim[t][e] * K.tis[4 * t + e];
					vi[e] = aim[t][e] * K.tic[4 * t + e] + are[t][e] * K.tis[4 * t + e];
				}
				u8* p = cs + 10240 + (16 * t + i16) * 160 + 32 * w + 8 * g;
				*(u32x2*)p = u32x2{pack2(vr[0], vr[1]), pack2(vr[2], vr[3])};
				*(u32x2*)(p + 5120) = u32x2{pack2(vi[0], vi[1]), pack2(vi[2], vi[3])};
			}
			HOOK(8);
			LBAR();
#pragma unroll
			for (int q = 0; q < 4; ++q) {
				const u8* p = cs + 10240 + (16 * (q >> 1) + i16) * 160 + 64 * (q & 1) + 16 * g;
				mr[q] = *(const u32x4*)p; mi[q] = *(const u32x4*)(p + 5120);
			}
			HOOK(9);
#pragma unroll
			for (int ks = 0; ks < 2; ++ks) {
#pragma unroll
				for (int t = 0; t < 2; ++t) {
					are[t] = mfma16(K.C1[ks], mr[2 * t + ks], ks ? are[t] : z4);
					aim[t] = mfma16(K.C1[ks], mi[2 * t + ks], ks ? aim[t] : z4);
				}
#pragma unroll
				for (int t = 0; t < 2; ++t) {
					are[t] = mfma16(N1[ks], mi[2 * t + ks], are[t]);
					aim[t] = mfma16(K.S1[ks], mr[2 * t + ks], aim[t]);
				}
				PINA(are[0]); PINA(are[1]); PINA(aim[0]); PINA(aim[1]);
				HOOK(10 + ks);
			}
#pragma unroll
			for (int t = 0; t < 2; ++t) {
				u8* p = cs + 20480 + (16 * t + i16) * 272 + 64 * w + 16 * g;
				*(f32x4*)p = are[t];
				*(f32x4*)(p + 8704) = aim[t];
			}
			HOOK(12);
			LBAR();
			{
				const u8* p = cs + 20480 + (tid >> 3) * 272 + (tid & 7) * 32;
				f32x4 r0 = *(const f32x4*)p, r1 = *(const f32x4*)(p + 16), i0 = *(const f32x4*)(p + 8704), i1 = *(const f32x4*)(p + 8720);
				const float im = b ? 1.0f : 0.0f;
				xr = u32x4{pack2c(fr[0] + r0[0], fr[1] + r0[1]), pack2c(fr[2] + r0[2], fr[3] + r0[3]), pack2c(fr[4] + r1[0], fr[5] + r1[1]), pack2c(fr[6] + r1[2], fr[7] + r1[3])};
				xi = u32x4{pack2c(fi[0] + im * i0[0], fi[1] + im * i0[1]), pack2c(fi[2] + im * i0[2], fi[3] + im * i0[3]),
						   pack2c(fi[4] + im * i1[0], fi[5] + im * i1[1]), pack2c(fi[6] + im * i1[2], fi[7] + im * i1[3])};
				swap32(xr, xi);
				st16(rs, xr, xo, o_co + b * 128);
				st16(rs, xi, xo, o_co + b * 128 + 8 * STRIP);
			}
			HOOK(13);
		}
		if (tid < 32) *(f32x4*)(sm + L_TAB + tid * 16) = f32x4{bflo(b1r[0]), bfhi(b1r[0]), bflo(b1r[1]), bfhi(b1r[1])};
		if (l == 0) {
			const unsigned fo = lane < 56 ? lane * 512 : OOB;
#pragma unroll
			for (int k = 0; k < 4; ++k) fb[k] = ld8(rs, fo + k * 128, O_FLAGB);
			SB();
			unsigned m = 0;
#pragma unroll
			for (int n = 0; n < 28; ++n) m = mx4(m, w1s[n]);
			if (UNLIKELY(anyp(m))) {
#pragma unroll 1
				for (int it = 0; it < SPIN; ++it) {
					__builtin_amdgcn_s_sleep(2);
					FENCE();
					m = 0;
#pragma unroll
					for (int n = 0; n < 28; ++n) { LDW1(n); m = mx4(m, w1s[n]); }
					if (!anyp(m)) break;
				}
			}
#pragma unroll
			for (int n = 0; n < 8; ++n) *(u32x4*)(sm + (w * 28 + n) * 1024 + l16) = w1s[n];
			SB();
#pragma unroll
			for (int k = 0; k < 4; ++k) check_flag(rs, fb[k], lane < 56, fo + k * 128, O_FLAGB, tag);
#pragma unroll
			for (int q = 0; q < 28; ++q) {
				LDW(q);
				if (q < 20) *(u32x4*)(sm + (w * 28 + 8 + q) * 1024 + l16) = w1s[8 + q];
				SB();
			}
		} else {
#pragma unroll
			for (int q = 28; q < 56; ++q) LDW(q);
			SB();
		}
		const unsigned o_xs = o_co + (4 * pp + w) * STRIP;
		{
			bool ok = lane >= 56;
#pragma unroll 1
			for (int it = 0; it < SPIN; ++it) {
				if (!ok) ok = ld4(rs, (56 * w + lane) * 128 + 124, o_xs) != POIS;
				if (__builtin_amdgcn_ballot_w64(!ok) == 0) break;
				__builtin_amdgcn_s_sleep(2);
				FENCE();
			}
			LBAR();
		}
		{
			u32x4 xs[28], B[28];
#define LDX(n) xs[n] = ld16(rs, l16 + ((n) & 3) * 1024, o_xs + ((n) & ~3) * 1024)
#pragma unroll
			for (int n = 0; n < 12; ++n) { LDX(n); SB(); }
			f32x16 ac[4];
			f32x4 gr[2];
			f32x16 z16;
#pragma unroll
			for (int r = 0; r < 16; ++r) z16[r] = 0.f;
			const f32x4 z4 = {0.f, 0.f, 0.f, 0.f};
			unsigned o_wp = L_TOP + w * 8192 + l16, o_tp = L_TOP + w * 8192 + (8 * h + q4) * 64 + (g & 1) * 32 + (pc & 1) * 16 + (pc >> 1) * 8;
			unsigned o_a1 = l16 + 57344, o_tab = L_TAB + h * 16;
			PIN(o_wp); PIN(o_tp); PIN(o_a1); PIN(o_tab);
			u8* wp = sm + o_wp;
			const u8* tp = sm + o_tp;
			const u8* ap[2] = {sm + l16, sm + o_a1};
#define LDA(q) A[(q) % NA] = *(const u32x4*)(ap[(q) / 56] + ((q) % 56) * 1024)
			constexpr int NA = 8, DA = 5;
			u32x4 A[NA];
			u32x2 tlo[4];
			float r2;
#pragma unroll
			for (int c = 0; c < 8; ++c) {
				if (c < 7) {
					unsigned m = 0;
#pragma unroll
					for (int k = 0; k < 4; ++k) m = mx4(m, xs[4 * c + k]);
					if (UNLIKELY(anyp(m))) {
#pragma unroll 1
						for (int it = 0; it < SPIN; ++it) {
							__builtin_amdgcn_s_sleep(2);
							FENCE();
							m = 0;
#pragma unroll
							for (int k = 0; k < 4; ++k) { LDX(4 * c + k); m = mx4(m, xs[4 * c + k]); }
							if (!anyp(m)) break;
						}
					}
				}
				SB();
#pragma unroll
				for (int i = 0; i < 8; ++i) {
					if (c > 0) {
						const int k = (c < 7) ? (i >> 1) : (i & 3), ks = 4 * (c - 1) + k;
						if ((c < 7) ? (i & 1) : (i >> 2)) {
							ac[0] = mfma32(A[ks % NA], B[ks], ks ? ac[0] : z16);
							PINA(ac[0]);
						} else {
							u32x2 m0 = {B[ks][0], B[ks][1]};
							if (ks == 0) m0[0] &= h ? 0xffffffffu : 0x0000ffffu;
							gr[0] = mfma4(m0, u32x2{B[ks][0], B[ks][1]}, ks ? gr[0] : z4);
							gr[1] = mfma4(u32x2{B[ks][2], B[ks][3]}, u32x2{B[ks][2], B[ks][3]}, ks ? gr[1] : z4);
							PINA(gr[0]); PINA(gr[1]);
						}
					}
					if (c < 7) {
#pragma unroll
						for (int o = 2 * i; o < 2 * i + 2; ++o) {
							if (o < 4) *(u32x4*)(wp + ((4 * c + o) & 7) * 1024) = xs[4 * c + o];
							else if (o < 12) {
								const int k = (o - 4) >> 1, ks = 4 * c + k;
								if (o & 1) B[ks] = cat(tlo[k], trread(tp + (ks & 7) * 1024 + 256));
								else tlo[k] = trread(tp + (ks & 7) * 1024);
							} else {
								LDA(4 * c + o - 12);
							}
						}
						if (i & 1) {
							const int n = 4 * c + 12 + (i >> 1);
							if (n < 28) LDX(n);
							else if (l == 0) LDW(n);
						}
					} else {
						if (i >= 4 && i - 4 < DA - 1) LDA(28 + i - 4);
						if (i == 6) {
							f32x4 s4 = gr[0] + gr[1];
							float sa = (lane & 1) ? s4[1] : s4[0], sb = (lane & 1) ? s4[3] : s4[2];
							int si = __builtin_bit_cast(int, (lane & 2) ? sb : sa);
							auto sw = __builtin_amdgcn_permlane32_swap(si, si, false, false);
							const unsigned s0 = sw[0], s1 = sw[1];
							float v = (__builtin_bit_cast(float, s0) + __builtin_bit_cast(float, s1)) * (1.0f / 448.0f);
							float r1 = __builtin_amdgcn_rsqf(v + EPS);
							r2 = r1 * __builtin_amdgcn_rsqf(__builtin_fmaf(v * r1, r1, EPS));
							PIN(r2);
						}
					}
					SB();
				}
			}
			const u8* tab = sm + o_tab;
			const unsigned o_hs = o_h + ((4 * pp + w) * 112 + 2 * ft) * 1024;
			GS gs;
#pragma unroll
			for (int st = 1; st < 5; ++st) {
#pragma unroll
				for (int q = 0; q < 4; ++q) gs.bb[q] = *(const f32x4*)(tab + (st - 1) * 128 + q * 32);
#pragma unroll
				for (int i = 0; i < 28; ++i) {
					const int q = 28 * st + i;
					if (st < 4) {
						ac[st] = mfma32(A[q % NA], B[i], i ? ac[st] : z16);
						PINA(ac[st]);
						if (q + DA - 1 < 112) LDA(q + DA - 1);
					}
					if (i >= 2 && i < 27) gelu_blk(gs, i - 2, ac[st - 1], r2);
					if (i == 14) st16(rs, u32x4{gs.o[0], gs.o[1], gs.o[2], gs.o[3]}, l16, o_hs + (st - 1) * 28 * 1024);
					if (i == 26) st16(rs, u32x4{gs.o[4], gs.o[5], gs.o[6], gs.o[7]}, l16 + 1024, o_hs + (st - 1) * 28 * 1024);
					if (l == 0 && st < 3 && (i & 1) && i < 16) LDW(40 + 8 * (st - 1) + (i >> 1));
					if (st == 3 && i >= HS0 && ((i - HS0) & 1) == 0 && (i - HS0) / 2 < 14) LDH((i - HS0) / 2);
					if (st == 4 && (i % 3) == 0 && (28 - HS0 + 1) / 2 + i / 3 < 14) LDH((28 - HS0 + 1) / 2 + i / 3);
					SB();
				}
			}
		}
		LBAR();
		{
			f32x16 acc[4], z16;
#pragma unroll
			for (int r = 0; r < 16; ++r) z16[r] = 0.f;
			u32x4 res[2], xq[14], qb[14];
			unsigned bias = 0;
			u8* ex = sm + L_TOP;
			u8* wq = sm + w * 16384 + l16;
			const u8* tq = sm + w * 16384 + (8 * h + q4) * 64 + (g & 1) * 32 + (pc & 1) * 16 + (pc >> 1) * 8;
			unsigned m;
#define H_CHECK(n0, n1) do { \
				m = 0; \
				_Pragma("unroll") for (int k = (n0); k < (n1); ++k) m = mx4(mx4(m, H[k][0]), H[k][1]); \
			} while (0)
#define H_RETRY(n0, n1) \
			if (UNLIKELY(anyp(m))) { \
				_Pragma("unroll 1") for (int it = 0; it < SPIN; ++it) { \
					__builtin_amdgcn_s_sleep(2); \
					FENCE(); \
					_Pragma("unroll") for (int k = 2 * (n0); k < 2 * (n1); ++k) LDH(k); \
					H_CHECK(n0, n1); \
					if (!anyp(m)) break; \
				} \
			}
			H_CHECK(0, 1);
			H_RETRY(0, 1)
			SB();
#pragma unroll
			for (int n = 0; n < 21; ++n) {
				acc[0] = mfma32(H[n][0], W[n][0], n ? acc[0] : z16);
				PINA(acc[0]);
				LDH(2 * n + 14);
				SB();
				acc[1] = mfma32(H[n][0], W[n][1], n ? acc[1] : z16);
				PINA(acc[1]);
				if (n < 20) H_CHECK(n + 1, n + 2);
				SB();
				acc[2] = mfma32(H[n][1], W[n][0], n ? acc[2] : z16);
				PINA(acc[2]);
				LDH(2 * n + 15);
				SB();
				acc[3] = mfma32(H[n][1], W[n][1], n ? acc[3] : z16);
				PINA(acc[3]);
				if (l == 0) {
					if (n == 8) bias = ld4(rs, (lane & 31) * 4, O_B2 + (64 * jt + 32 * (w & 1)) * 4);
					if (n == 9 || n == 10) res[n - 9] = ld16(rs, (lane & 31) * 64 + h * 16 + (n - 9) * 32, O_XT1 + (2 * pt + (w >> 1)) * STRIP + (64 * jt + 32 * (w & 1)) * 64);
				} else {
					if (n < 7) {
						xq[n] = ld16(rs, l16, O_XT3 + (2 * pt + (w >> 1)) * STRIP + (7 * w + n) * 1024);
					} else if (n < 14) {
						xq[n] = ld16(rs, l16, O_XT3 + (2 * pt + ((w >> 1) ^ 1)) * STRIP + (7 * w + n - 7) * 1024);
						*(u32x4*)(wq + (n - 7) * 1024) = xq[n - 7];
						qb[n - 7] = ld16(rs, l16, O_QT + ((2 * jt + (w & 1)) * 28 + 7 * w + n - 7) * 1024);
					} else {
						*(u32x4*)(wq + (n - 7) * 1024) = xq[n - 7];
						qb[n - 7] = ld16(rs, l16, O_QT + ((2 * jt + ((w & 1) ^ 1)) * 28 + 7 * w + n - 14) * 1024);
						xq[n - 14] = cat(trread(tq + (n - 14) * 1024), trread(tq + (n - 14) * 1024 + 256));
					}
					if (n == 6) bias = (unsigned)*(const u16*)(b2 + (64 * jt + 32 * (w & 1) + (lane & 31)) * 2) << 16;
				}
				SB();
				if (n < 20) { H_RETRY(n + 1, n + 2) }
			}
			if (l == 1) {
#pragma unroll
				for (int i = 0; i < 14; ++i) {
					acc[2 * (i / 7)] = mfma32(xq[i], qb[i % 7], acc[2 * (i / 7)]);
					PINA(acc[2 * (i / 7)]);
					if (i < 7) xq[7 + i] = cat(trread(tq + (7 + i) * 1024), trread(tq + (7 + i) * 1024 + 256));
					SB();
					acc[2 * (i / 7) + 1] = mfma32(xq[i], qb[7 + i % 7], acc[2 * (i / 7) + 1]);
					PINA(acc[2 * (i / 7) + 1]);
					SB();
				}
			}
			H_CHECK(21, 28);
			H_RETRY(21, 28)
			SB();
#pragma unroll
			for (int a = 1; a < 5; ++a) {
#pragma unroll
				for (int n = 21; n < 28; ++n) {
					acc[a & 3] = mfma32(H[n][(a >> 1) & 1], W[n][a & 1], acc[a & 3]);
					PINA(acc[a & 3]);
					if (a > 1 && n >= 23 && n < 27) {
						const int q = n - 23, i = a - 1;
						*(f32x4*)(ex + (w ^ i) * 12288 + (i - 1) * 4096 + l16 + q * 1024) = f32x4{acc[i][4 * q], acc[i][4 * q + 1], acc[i][4 * q + 2], acc[i][4 * q + 3]};
					}
					SB();
				}
			}
			LBAR();
			float v[16];
			const float bf = __builtin_bit_cast(float, bias);
#pragma unroll
			for (int q = 0; q < 4; ++q) {
				const u8* p = ex + w * 12288 + q * 1024 + l16;
				f32x4 s = (*(const f32x4*)p + *(const f32x4*)(p + 4096)) + *(const f32x4*)(p + 8192);
#pragma unroll
				for (int e = 0; e < 4; ++e) {
					v[4 * q + e] = (acc[0][4 * q + e] + bf) + s[e];
					if (l == 0) v[4 * q + e] += (e & 1) ? bfhi(res[q >> 1][2 * (q & 1) + (e >> 1)]) : bflo(res[q >> 1][2 * (q & 1) + (e >> 1)]);
				}
			}
			u32x4 X = {pack2c(v[0], v[1]), pack2c(v[2], v[3]), pack2c(v[4], v[5]), pack2c(v[6], v[7])};
			u32x4 Y = {pack2c(v[8], v[9]), pack2c(v[10], v[11]), pack2c(v[12], v[13]), pack2c(v[14], v[15])};
			if (l == 0) {
				swap16(X, Y);
				const unsigned o = O_XT2 + (2 * pt + (w >> 1)) * STRIP + (64 * jt + 32 * (w & 1)) * 64;
				const unsigned vo = i16 * 64 + (g & 1) * 32 + h * 16;
				st16(rs, X, vo, o);
				st16(rs, Y, vo + 1024, o);
				SB();
				const float keep = (64 * jt + 32 * (w & 1) + (lane & 31) == 1) ? 0.f : 1.f;
				float s[8];
#pragma unroll
				for (int i = 0; i < 8; ++i) {
					int a = __builtin_bit_cast(int, v[i] * v[i] * keep), c = __builtin_bit_cast(int, v[i + 8] * v[i + 8] * keep);
					auto r = __builtin_amdgcn_permlane16_swap(a, c, false, false);
					const unsigned r0 = r[0], r1 = r[1];
					s[i] = __builtin_bit_cast(float, r0) + __builtin_bit_cast(float, r1);
				}
#pragma unroll
				for (int i = 0; i < 4; ++i) s[i] = ((lane & 8) ? s[i + 4] : s[i]) + dpp<0x128>((lane & 8) ? s[i] : s[i + 4]);
#pragma unroll
				for (int i = 0; i < 2; ++i) s[i] = ((lane & 4) ? s[i + 2] : s[i]) + dpp<0x141>((lane & 4) ? s[i] : s[i + 2]);
				s[0] = ((lane & 2) ? s[1] : s[0]) + dpp<0x4e>((lane & 2) ? s[0] : s[1]);
				s[0] += dpp<0xb1>(s[0]);
				if (!(lane & 1)) *(float*)(ex + w * 12288 + 11264 + (16 * (g & 1) + 8 * h + ((lane >> 1) & 7)) * 4) = s[0];
				LBAR();
				if (tid < 8) {
					const u8* p = ex + (tid >> 2) * 24576 + 11264 + (tid & 3) * 32;
					f32x4 s0 = *(const f32x4*)p + *(const f32x4*)(p + 12288), s1 = *(const f32x4*)(p + 16) + *(const f32x4*)(p + 12304);
					st16(rs, u32x4{pack2c(s0[0], s0[1]), pack2c(s0[2], s0[3]), pack2c(s1[0], s1[1]), pack2c(s1[2], s1[3])}, tid * 16, O_SS1 + jt * 4096 + pt * 128);
				}
				const u32x2 fp = ld8(rs, lane < 32 ? lane * 128 : OOB, O_FLAGP);
				SB();
				int ln = lane;
				PIN(ln);
#pragma unroll
				for (int part = 0; part < 24; ++part) col_part(K, part, w, ln >> 4, ln & 15);
				check_flag(rs, fp, lane < 32, lane * 128, O_FLAGP, tag);
			} else {
				u8* im = ex + w * 12288;
				*(u32x4*)(im + (lane & 31) * 64 + h * 16) = X;
				*(u32x4*)(im + (lane & 31) * 64 + h * 16 + 32) = Y;
#pragma unroll
				for (int u = 0; u < 2; ++u) {
					const u8* p = im + (8 * g + q4) * 64 + 32 * u + 8 * pc;
					*(u32x4*)(out + (32 * (32 * (w >> 1) + 16 * u + i16) + pt) * 896 + (64 * jt + 32 * (w & 1) + 8 * g) * 2) = cat(trread(p), trread(p + 256));
				}
			}
		}
	};
	layer(IC<0>{});
	layer(IC<1>{});
}

extern "C" void kernel_launch(void* const* d_in, const int* in_sizes, int n_in, void* d_out, int out_size, void* d_ws, size_t ws_size,
		hipStream_t stream) {
	k_all<<<256, 256, 0, stream>>>((const u8*)d_in[0], (const u8*)d_in[1], (const u8*)d_in[2], (const u8*)d_in[4], (const u8*)d_in[5],
		(const u8*)d_in[6], (const u8*)d_in[7], (u8*)d_out, (u8*)d_ws);
}
```

```cpp
#include <hip/hip_runtime.h>
#include <hip/hip_bf16.h>
#include <stdint.h>

typedef __bf16 bf16x8 __attribute__((ext_vector_type(8)));
typedef __bf16 bf16x2 __attribute__((ext_vector_type(2)));
typedef short s16x4 __attribute__((ext_vector_type(4)));
typedef float f32x2 __attribute__((ext_vector_type(2)));
typedef float f32x4 __attribute__((ext_vector_type(4)));
typedef float f32x16 __attribute__((ext_vector_type(16)));
typedef unsigned u32x2 __attribute__((ext_vector_type(2)));
typedef unsigned u32x4 __attribute__((ext_vector_type(4)));
typedef u32x4 u32x4u __attribute__((aligned(2)));
typedef unsigned char u8;
typedef unsigned short u16;
typedef __amdgpu_buffer_rsrc_t rsrc_t;
template <int V> struct IC { static constexpr int value = V; };
#define LAS __attribute__((address_space(3)))
#define DEV __device__ __forceinline__

constexpr unsigned STRIP = 28672u;
constexpr unsigned XT_B = 64u * STRIP;
constexpr unsigned FR_B = 1605632u;
constexpr unsigned H_B = 7340032u;
constexpr unsigned O_FLAGA = 0, O_FLAGB = 32768, O_FLAGP = 61440, O_EPOCH = 65536;
constexpr unsigned O_B2 = 66560;
constexpr unsigned O_SS0 = 70656, O_SS1 = 74752;
constexpr unsigned O_XT0 = 131072;
constexpr unsigned O_XT1 = O_XT0 + XT_B;
constexpr unsigned O_XT2 = O_XT1 + XT_B;
constexpr unsigned O_H1 = O_XT2 + XT_B;
constexpr unsigned O_XT3 = O_H1 + H_B;
constexpr unsigned O_H2 = O_XT3 + XT_B;
constexpr unsigned O_W1F = O_H2 + H_B;
constexpr unsigned O_W2F = O_W1F + FR_B;
constexpr unsigned O_W2O = O_W2F + FR_B;
constexpr unsigned O_QT = O_W2O + FR_B;
constexpr unsigned O_WT = O_QT + 401408;
constexpr unsigned WS_END = O_WT + XT_B;
constexpr unsigned L_TOP = 114688;
constexpr unsigned L_TAB = L_TOP + 48640;

constexpr float EPS = 1e-5f;
constexpr float SC1 = 0.04724555912615340f;
constexpr float SC2 = 0.06681531047810609f;
constexpr unsigned POIS = 0xffffffffu;
constexpr unsigned OOB = 0x80000000u;
#define SPINB 18
constexpr int SPIN = 1 << SPINB;

#define PROF_INIT() unsigned long long g_t0; asm volatile("s_memrealtime %0\n\ts_waitcnt lgkmcnt(0)" : "=s"(g_t0))
#define STAMP(i) do { if ((i) == 25) { for (int z_ = 0; z_ < 100000; ++z_) { unsigned long long t_; asm volatile("s_memrealtime %0\n\ts_waitcnt lgkmcnt(0)" : "=s"(t_)); if (t_ - g_t0 >= 8000ull) break; __builtin_amdgcn_s_sleep(1); } } } while (0)
#define FLUSH()
#define CNT(i)

DEV unsigned pack2(float a, float b) {
	f32x2 v = {a, b};
	return __builtin_bit_cast(unsigned, __builtin_convertvector(v, bf16x2));
}
DEV unsigned pack2c(float a, float b) { return min(pack2(a, b), 0xfffffffeu); }
DEV float bflo(unsigned u) { return __builtin_bit_cast(float, u << 16); }
DEV float bfhi(unsigned u) { return __builtin_bit_cast(float, u & 0xffff0000u); }
DEV f32x4 mfma16(u32x4 a, u32x4 b, f32x4 c) {
	return __builtin_amdgcn_mfma_f32_16x16x32_bf16(__builtin_bit_cast(bf16x8, a), __builtin_bit_cast(bf16x8, b), c, 0, 0, 0);
}
DEV f32x16 mfma32(u32x4 a, u32x4 b, f32x16 c) {
	return __builtin_amdgcn_mfma_f32_32x32x16_bf16(__builtin_bit_cast(bf16x8, a), __builtin_bit_cast(bf16x8, b), c, 0, 0, 0);
}
DEV f32x4 mfma4(u32x2 a, u32x2 b, f32x4 c) {
	return __builtin_amdgcn_mfma_f32_4x4x4bf16_1k(__builtin_bit_cast(s16x4, a), __builtin_bit_cast(s16x4, b), c, 0, 0, 0);
}
DEV rsrc_t mkrs(const void* p, unsigned bytes) { return __builtin_amdgcn_make_buffer_rsrc((void*)p, 0, bytes, 0x00020000); }
DEV void dma16(rsrc_t rs, u8* lds, unsigned voff, unsigned soff) {
	__builtin_amdgcn_raw_ptr_buffer_load_lds(rs, (LAS void*)lds, 16, voff, soff, 0, 0);
}
DEV u32x4 ld16(rsrc_t rs, unsigned v, unsigned s) { return __builtin_amdgcn_raw_buffer_load_b128(rs, v, s, 16); }
DEV u32x2 ld8(rsrc_t rs, unsigned v, unsigned s) { return __builtin_amdgcn_raw_buffer_load_b64(rs, v, s, 16); }
DEV unsigned ld4(rsrc_t rs, unsigned v, unsigned s) { return __builtin_amdgcn_raw_buffer_load_b32(rs, v, s, 16); }
DEV void st16(rsrc_t rs, u32x4 d, unsigned v, unsigned s) { __builtin_amdgcn_raw_buffer_store_b128(d, rs, v, s, 16); }
DEV void st8(rsrc_t rs, u32x2 d, unsigned v, unsigned s) { __builtin_amdgcn_raw_buffer_store_b64(d, rs, v, s, 16); }
DEV void st4(rsrc_t rs, unsigned d, unsigned v, unsigned s) { __builtin_amdgcn_raw_buffer_store_b32(d, rs, v, s, 16); }
DEV u32x2 trread(const u8* lds) {
	return __builtin_bit_cast(u32x2, __builtin_amdgcn_ds_read_tr16_b64_v4i16((LAS s16x4*)lds));
}
DEV u32x4 cat(u32x2 a, u32x2 b) { return u32x4{a[0], a[1], b[0], b[1]}; }
DEV float cosr(float x) { return __builtin_amdgcn_cosf(x); }
DEV float sinr(float x) { return __builtin_amdgcn_sinf(x); }
DEV unsigned mx4(unsigned m, u32x4 v) { return max(max(max(m, v[0]), v[1]), max(max(v[2], v[3]), 0u)); }
DEV bool anyp(unsigned m) { return __builtin_amdgcn_ballot_w64(m == POIS) != 0; }
DEV void swap32(u32x4& a, u32x4& b) {
#pragma unroll
	for (int i = 0; i < 4; ++i) {
		auto r = __builtin_amdgcn_permlane32_swap(a[i], b[i], false, false);
		a[i] = r[0]; b[i] = r[1];
	}
}
DEV void swap16(u32x4& a, u32x4& b) {
#pragma unroll
	for (int i = 0; i < 4; ++i) {
		auto r = __builtin_amdgcn_permlane16_swap(a[i], b[i], false, false);
		a[i] = r[0]; b[i] = r[1];
	}
}
#define WAIT_BAR(N) asm volatile("s_waitcnt vmcnt(" #N ") lgkmcnt(0)\n\ts_barrier" ::: "memory")
#define LBAR() asm volatile("s_waitcnt lgkmcnt(0)\n\ts_barrier" ::: "memory")
#define FENCE() asm volatile("" ::: "memory")
#define PIN(x) asm volatile("" : "+v"(x))
#define PINA(x) asm volatile("" : "+a"(x))
#define WAITV(N) asm volatile("s_waitcnt vmcnt(" #N ")" ::: "memory")
#define SB() __builtin_amdgcn_sched_barrier(0)
#define UNLIKELY(x) __builtin_expect(!!(x), 0)

DEV void poll_flag(rsrc_t rs, bool act, unsigned v, unsigned s, u32x2 tag) {
	bool ok = !act;
#pragma unroll 1
	for (int it = 0; it < SPIN; ++it) {
		if (!ok) {
			u32x2 a = ld8(rs, v, s);
			ok = (a[0] == tag[0]) & (a[1] == tag[1]);
		}
		if (__builtin_amdgcn_ballot_w64(!ok) == 0) break;
		__builtin_amdgcn_s_sleep(2);
		FENCE();
	}
	FENCE();
}

DEV void basis(int j, float& kq, float& ph) {
	int c = j >> 1;
	int kk = c ? c : ((j & 1) ? 0 : 224);
	kq = (float)kk * (1.0f / 448.0f);
	ph = (c && (j & 1)) ? 0.25f : 0.0f;
}

struct ColK {
	u32x4 C1[2], S1[2], C2[2], S2[2];
	float twc[8], tws[8], tic[8], tis[8];
};
DEV void col_part(ColK& K, int part, int w, int g, int i16) {
	const float f1 = (float)(16 * w + i16);
	if (part < 8) {
		const int ks = part >> 2, i = part & 3;
		float a = f1 * (float)(32 * ks + 8 * g + 2 * i) * (1.0f / 64.0f), bb = a + f1 * (1.0f / 64.0f);
		unsigned c = pack2(cosr(a), cosr(bb)), s = pack2(sinr(a), sinr(bb));
		PIN(c); PIN(s);
		K.C1[ks][i] = c; K.S1[ks][i] = s;
	} else if (part < 16) {
		const int t2 = (part >> 2) & 1, i = part & 3;
		float m = (float)(16 * t2 + i16) * (1.0f / 32.0f);
		float a = m * (float)(16 * (i >> 1) + 4 * g + 2 * (i & 1)), bb = a + m;
		unsigned c = pack2(cosr(a), cosr(bb)), s = pack2(sinr(a), sinr(bb));
		PIN(c); PIN(s);
		K.C2[t2][i] = c; K.S2[t2][i] = s;
	} else {
		const int i = part & 7;
		float a = f1 * (float)(16 * (i >> 2) + 4 * g + (i & 3)) * (1.0f / 2048.0f);
		K.twc[i] = cosr(a);
		K.tws[i] = sinr(a);
		float bb = (float)(16 * w + 4 * g + (i & 3)) * (float)(16 * (i >> 2) + i16) * (1.0f / 2048.0f);
		K.tic[i] = cosr(bb) * (1.0f / 2048.0f);
		K.tis[i] = sinr(bb) * (1.0f / 2048.0f);
		PIN(K.twc[i]); PIN(K.tws[i]); PIN(K.tic[i]); PIN(K.tis[i]);
	}
}

struct GS {
	float pre[2], t[2];
	unsigned o[8];
	f32x4 bb[4];
};
DEV void gelu_blk(GS& s, int k, const f32x16& acc, float r) {
	const int p = k / 3, j = k % 3;
	if (j == 0) {
		if (p) {
			s.o[p - 1] = pack2c(s.t[0], s.t[1]);
			PIN(s.o[p - 1]);
		}
		if (p < 8) {
			s.pre[0] = __builtin_fmaf(acc[2 * p], r, s.bb[p >> 1][(2 * p) & 3]);
			s.pre[1] = __builtin_fmaf(acc[2 * p + 1], r, s.bb[p >> 1][(2 * p + 1) & 3]);
			s.t[0] = __builtin_fmaf(s.pre[0] * s.pre[0], -0.10294324f, -2.30220820f);
			s.t[1] = s.pre[1] * s.pre[1];
			asm volatile("" : "+v"(s.pre[0]), "+v"(s.pre[1]), "+v"(s.t[0]), "+v"(s.t[1]));
		}
	} else if (j == 1) {
		s.t[1] = __builtin_fmaf(s.t[1], -0.10294324f, -2.30220820f);
		s.t[0] = __builtin_amdgcn_exp2f(s.pre[0] * s.t[0]);
		s.t[1] = __builtin_amdgcn_exp2f(s.pre[1] * s.t[1]);
		asm volatile("" : "+v"(s.t[0]), "+v"(s.t[1]));
	} else {
		s.t[0] = s.pre[0] * __builtin_amdgcn_rcpf(1.0f + s.t[0]);
		s.t[1] = s.pre[1] * __builtin_amdgcn_rcpf(1.0f + s.t[1]);
		asm volatile("" : "+v"(s.t[0]), "+v"(s.t[1]));
	}
}
template <int CTRL> DEV float dpp(float x) {
	return __builtin_bit_cast(float, __builtin_amdgcn_update_dpp(0, __builtin_bit_cast(int, x), CTRL, 0xf, 0xf, true));
}

#define VA 26
#define HS0 16

DEV void check_flag(rsrc_t rs, u32x2 a, bool act, unsigned v, unsigned s, u32x2 tag) {
	const bool bad = act & ((a[0] != tag[0]) | (a[1] != tag[1]));
	if (UNLIKELY(__builtin_amdgcn_ballot_w64(bad) != 0)) poll_flag(rs, bad, v, s, tag);
	FENCE();
}
template <int HI> DEV void p0_gemm(const u8* sm, const u32x4 (&qf)[14], f32x4 (&acc)[4], int g, int i16) {
	unsigned sg = 2 * (i16 >> 2) + ((i16 >> 1) & 1);
	const u8* base = sm + (8 * (i16 >> 2) + (i16 & 3)) * 896;
	const u8* bp[4] = {base + ((g ^ sg) * 16), base + (((4 + g) ^ sg) * 16), base + ((g ^ sg) * 16) + HI, base + (((4 + g) ^ sg) * 16) + HI};
	u32x4 a[8];
#pragma unroll
	for (int i = -8; i < 56; ++i) {
		if (i >= 0) {
			acc[i & 3] = mfma16(a[i & 7], qf[i >> 2], i < 4 ? f32x4{0.f, 0.f, 0.f, 0.f} : acc[i & 3]);
			PINA(acc[i & 3]);
		}
		const int n = i + 8, ks = n >> 2, t = n & 3;
		if (n < 56) a[n & 7] = *(const u32x4*)(bp[(ks & 1) + (t & 2)] + (ks >> 1) * 128 + (t & 1) * 3584);
		SB();
	}
}
DEV float dot2(unsigned a, unsigned b, float c) {
	return __builtin_amdgcn_fdot2_f32_bf16(__builtin_bit_cast(bf16x2, a), __builtin_bit_cast(bf16x2, b), c, false);
}
__global__ __launch_bounds__(256, 1) void k_all(const u8* __restrict__ x, const u8* __restrict__ wr, const u8* __restrict__ wi,
		const u8* __restrict__ w1, const u8* __restrict__ b1, const u8* __restrict__ w2, const u8* __restrict__ b2,
		u8* out, u8* ws) {
	__shared__ __attribute__((aligned(16))) u8 sm[163840];
	const int tid = threadIdx.x, lane = tid & 63;
	int w = __builtin_amdgcn_readfirstlane(tid >> 6);
	asm volatile("" : "+s"(w));
	const int g = lane >> 4, i16 = lane & 15, h = lane >> 5;
	const int q4 = (lane >> 2) & 3, pc = lane & 3;
	const int b = blockIdx.x;
	const rsrc_t rs = mkrs(ws, WS_END);
	const u32x4 pois = {POIS, POIS, POIS, POIS};
	PROF_INIT();
	STAMP(0);
	const u32x2 ep = ld8(rs, 0, O_EPOCH);
	u32x2 tag;
	if (b >= 224) {
		const int sb = b - 224;
		const int li = lane & 31;
		u32x4 v[16];
		if (li < 29) {
			const u8* src = ((w >> 1) ? wi : wr) + (2 * sb + (w & 1) + 64 * h) * 450 + (li < 28 ? 16 * li : 434);
#pragma unroll
			for (int n = 0; n < 16; ++n) v[n] = *(const u32x4u*)(src + n * (128 * 450));
		}
		SB();
		const unsigned long long tg = (((unsigned long long)ep[1] << 32) | ep[0]) + 0x9e3779b97f4a7c15ull;
		tag = u32x2{(unsigned)tg, (unsigned)(tg >> 32)};
		st8(rs, tag, tid ? OOB : 0u, O_FLAGA + b * 128);
		SB();
		if (li < 29) {
			u8* dst = sm + (32 * w + h) * 464;
#pragma unroll
			for (int n = 0; n < 16; ++n) {
				*(u32x4*)(dst + n * 928 + li * 16) = v[n];
				if (li == 28) *(u16*)(dst + n * 928) = (u16)(v[n][3] >> 16);
			}
		}
		poll_flag(rs, tid < 224, tid * 128, O_FLAGA, tag);
		LBAR();
#pragma unroll
		for (int n = 0; n < 7; ++n) {
			const int task = w + 4 * n, cgp = task >> 1, fl = task & 1;
#pragma unroll
			for (int t = 0; t < 2; ++t) {
				const u8* p = sm + (fl * 32 + 16 * t + 4 * g + q4) * 464 + cgp * 32 + pc * 8;
				u32x2 re = trread(p), im = trread(p + 64 * 464);
				u32x4 o = {min(__builtin_amdgcn_perm(im[0], re[0], 0x05040100u), 0xfffffffeu), min(__builtin_amdgcn_perm(im[0], re[0], 0x07060302u), 0xfffffffeu),
						   min(__builtin_amdgcn_perm(im[1], re[1], 0x05040100u), 0xfffffffeu), min(__builtin_amdgcn_perm(im[1], re[1], 0x07060302u), 0xfffffffeu)};
				st16(rs, o, i16 * 8192 + g * 16 + t * 64, O_WT + cgp * 131072 + (2 * sb + fl) * 128);
			}
		}
		STAMP(1);
#pragma unroll 1
		for (int fr = sb * 4 + w; fr < 392; fr += 128) {
			int dt = fr / 28, ks = fr % 28;
			float d = (float)(32 * dt + li);
			float c[8];
#pragma unroll
			for (int i = 0; i < 8; ++i) {
				int j = 16 * ks + 8 * h + i;
				float kq, ph;
				basis(j, kq, ph);
				c[i] = cosr(__builtin_fmaf(kq, d, ph)) * ((j < 2) ? SC1 : SC2);
			}
			st16(rs, u32x4{pack2(c[0], c[1]), pack2(c[2], c[3]), pack2(c[4], c[5]), pack2(c[6], c[7])}, lane * 16, O_QT + fr * 1024);
		}
#pragma unroll 2
		for (int n = 0; n < 70; ++n) st16(rs, pois, tid * 16, O_XT3 + (sb * 70 + n) * 4096);
		WAIT_BAR(0);
		if (tid == 0) st8(rs, tag, 0, O_FLAGP + sb * 128);
		STAMP(2);
		FLUSH();
		return;
	}
	const int xcd = b & 7, slot = b >> 3, s7 = slot / 7, jt = slot % 7;
	const int pp = 4 * (xcd >> 1) + s7, ft = 7 * (xcd & 1) + jt, pt = 2 * pp + (xcd & 1);
	ColK K;
	u32x2 b1r;
	{
		const int p0 = 4 * xcd + s7;
		const bool fold = p0 < 28;
		const int pf = fold ? p0 : p0 - 28;
		u8* bufW = sm + 57344;
		const unsigned o_xt0 = O_XT0 + 2 * p0 * STRIP + (64 * jt + 16 * w) * 64, v_xt0 = i16 * 64 + g * 16;
		const unsigned o_ss0 = O_SS0 + (64 * p0 + 16 * jt + 4 * w) * 2, v_ss0 = (lane == 0 && jt < 4) ? 0u : OOB;
		const unsigned w1f_o = O_W1F + (2 * pf * 28 + 4 * jt + w) * 1024, v_f = fold ? lane * 16 : OOB;
		st16(rs, pois, v_xt0, o_xt0);
		st16(rs, pois, v_xt0, o_xt0 + STRIP);
		st8(rs, u32x2{POIS, POIS}, v_ss0, o_ss0);
		st16(rs, pois, tid * 16, O_WT + b * 8192);
		st16(rs, pois, tid * 16, O_WT + b * 8192 + 4096);
		st16(rs, pois, v_f, w1f_o);
		st16(rs, pois, v_f, w1f_o + 28 * 1024);
		SB();
		const rsrc_t rx = mkrs(x, 2048u * 896u), r1 = mkrs(w1, 448u * 3584u), r2 = mkrs(w2, 1792u * 896u);
		const int vw[2] = {(lane >> 3) * 3584 + (((lane & 7) ^ (((lane >> 4) & 1) << 1)) * 16), (lane >> 3) * 3584 + (((lane & 7) ^ ((((lane >> 4) & 1) | 2) << 1)) * 16)};
		unsigned vx[7], v2[7];
		u32x4 w2r[7];
		const int j = 64 * jt + 16 * w + i16;
		float kq, ph;
		basis(j, kq, ph);
		const float a0 = (float)(8 * g) * kq + ph;
		u32x4 qf[14];
#pragma unroll
		for (int v = 0; v < 42; ++v) {
			if (v < 14) {
				const int n = v % 7;
				if (v < 7) {
					const int up = (8 * n + lane >= 56), dr = n + up;
					const unsigned c = ((unsigned)(8 * n + lane - 56 * up) ^ (unsigned)(2 * w + ((dr >> 1) & 1))) * 16;
					vx[n] = dr * 28672 + c;
					v2[n] = dr * 896 + c;
				}
				const int r = 32 * (v / 7) + 8 * w;
				dma16(rx, sm + r * 896 + n * 1024, vx[n], p0 * 896u + r * 28672u);
			} else if (v < 28) {
				const int i = 14 * w + v - 14;
				dma16(r1, bufW + i * 1024, vw[v & 1], i * (8 * 3584) + pf * 128);
			} else if (v < 35) {
				dma16(r2, sm + L_TOP + 8 * w * 896 + (v - 28) * 1024, v2[v - 28], (64 * pf + 16 * w) * 896u);
			} else {
				w2r[v - 35] = __builtin_amdgcn_raw_buffer_load_b128(r2, lane * 16, (64 * pf + 16 * w + 8) * 896u + (v - 35) * 1024, 0);
				if (v == 41) dma16(mkrs(b2, 896u), sm + L_TOP + 28672, lane * 16, 0);
			}
			SB();
			if (v < 28) {
#pragma unroll
				for (int i = 2 * (v & 1); i < 2 * (v & 1) + 2; ++i) {
					const int ks = v >> 1;
					unsigned c = pack2(cosr(__builtin_fmaf(kq, (float)(32 * ks + 2 * i), a0)), cosr(__builtin_fmaf(kq, (float)(32 * ks + 2 * i + 1), a0)));
					PIN(c);
					qf[ks][i] = c;
				}
			} else {
#pragma unroll
				for (int part = (v - 28) * 12 / 7; part < (v - 27) * 12 / 7; ++part) col_part(K, part, w, g, i16);
			}
			if (v == VA) {
				u32x2 e2 = ep;
				PIN(e2);
				const unsigned long long tg = (((unsigned long long)e2[1] << 32) | e2[0]) + 0x9e3779b97f4a7c15ull;
				tag = u32x2{(unsigned)tg, (unsigned)(tg >> 32)};
				asm volatile("s_waitcnt vmcnt(%0)\n\ts_barrier" ::"n"(VA + 1) : "memory");
				st8(rs, tag, tid ? OOB : 0u, O_FLAGA + b * 128);
			}
			SB();
		}
		b1r = *(const u32x2*)(b1 + (128 * ft + 4 * (lane & 31)) * 2);
		const u32x2 fa = ld8(rs, tid * 128, O_FLAGA);
		const float scj = (j < 2) ? SC1 : SC2;
		STAMP(1);
		WAIT_BAR(32);
		STAMP(2);
		f32x4 acc[4];
		p0_gemm<28672>(sm, qf, acc, g, i16);
#pragma unroll
		for (int u = 0; u < 2; ++u) {
			u32x4 o = {pack2c(acc[2 * u][0] * scj, acc[2 * u][1] * scj), pack2c(acc[2 * u][2] * scj, acc[2 * u][3] * scj),
					   pack2c(acc[2 * u + 1][0] * scj, acc[2 * u + 1][1] * scj), pack2c(acc[2 * u + 1][2] * scj, acc[2 * u + 1][3] * scj)};
			st16(rs, o, v_xt0, o_xt0 + u * STRIP);
		}
		SB();
		STAMP(3);
		{
			const u8* rb = sm + (16 * (jt & 3) + 4 * w + g) * 896 + i16 * 16;
			float s1 = 0.f, s2 = 0.f;
#pragma unroll
			for (int q = 0; q < 4; ++q) {
				u32x4 v = *(const u32x4*)(rb + q * 256);
#pragma unroll
				for (int i = 0; i < 4; ++i) {
					unsigned e = v[i];
					if (q == 3) e = (i16 < 8) ? e : 0u;
					s1 = dot2(e, 0x3f803f80u, s1);
					s2 = dot2(e, e, s2);
				}
			}
			s1 += dpp<0x128>(s1); s2 += dpp<0x128>(s2);
			s1 += dpp<0x124>(s1); s2 += dpp<0x124>(s2);
			s1 += dpp<0x122>(s1); s2 += dpp<0x122>(s2);
			s1 += dpp<0x121>(s1); s2 += dpp<0x121>(s2);
			int sv = __builtin_bit_cast(int, fmaxf(s2 - s1 * s1 * (1.0f / 448.0f), 0.f));
			float t0 = __builtin_bit_cast(float, __builtin_amdgcn_readlane(sv, 0)), t1 = __builtin_bit_cast(float, __builtin_amdgcn_readlane(sv, 16));
			float t2 = __builtin_bit_cast(float, __builtin_amdgcn_readlane(sv, 32)), t3 = __builtin_bit_cast(float, __builtin_amdgcn_readlane(sv, 48));
			st8(rs, u32x2{pack2c(t0, t1), pack2c(t2, t3)}, v_ss0, o_ss0);
		}
		WAIT_BAR(3);
		STAMP(4);
#pragma unroll
		for (int n = 0; n < 7; ++n) *(u32x4*)(sm + 8 * w * 896 + v2[n]) = w2r[n];
		SB();
		{
			const unsigned kap = ((q4 >> 1) & 1) | ((g & 1) << 1);
			const u8* rb = bufW + (8 * g + q4) * 128 + (pc & 1) * 8;
			const u8* bp[4];
#pragma unroll
			for (int t = 0; t < 4; ++t) bp[t] = rb + (((2 * t + (pc >> 1)) ^ (kap << 1)) * 16);
			const unsigned xo = (16 * w + ((lane & 31) >> 2)) * STRIP + h * 64 + pc * 16;
			u32x2 lo[8], hi[8];
#pragma unroll
			for (int i = -8; i < 56; ++i) {
				if (i >= 0) {
					acc[i & 3] = mfma16(qf[i >> 2], cat(lo[i & 7], hi[i & 7]), i < 4 ? f32x4{0.f, 0.f, 0.f, 0.f} : acc[i & 3]);
					PINA(acc[i & 3]);
				}
				const int n = i + 8;
				if (n < 56) {
					lo[n & 7] = trread(bp[n & 3] + (n >> 2) * 4096);
					hi[n & 7] = trread(bp[n & 3] + (n >> 2) * 4096 + 512);
				}
				if (i >= 0 && (i & 3) == 0) {
					const int k = i >> 2;
					if (k < 10) st16(rs, pois, tid * 16, O_XT2 + (b * 10 + k) * 4096);
					else if (k < 12) st16(rs, pois, xo, O_XT1 + b * 128 + (k - 10) * 8 * STRIP);
					else if (k == 12) st16(rs, pois, tid < 8 ? tid * 16 : OOB, O_SS1 + b * 128);
				}
				SB();
			}
			const int j0 = 64 * jt + 16 * w + 4 * g;
			const float s0 = (j0 == 0) ? SC1 : SC2, s1 = (j0 == 0) ? 0.f : SC2;
#pragma unroll
			for (int u = 0; u < 2; ++u) {
				unsigned x0 = pack2c(acc[2 * u][0] * s0, acc[2 * u][1] * s1), x1 = pack2c(acc[2 * u][2] * SC2, acc[2 * u][3] * SC2);
				unsigned y0 = pack2c(acc[2 * u + 1][0] * s0, acc[2 * u + 1][1] * s1), y1 = pack2c(acc[2 * u + 1][2] * SC2, acc[2 * u + 1][3] * SC2);
				auto r0 = __builtin_amdgcn_permlane16_swap(x0, y0, false, false);
				auto r1v = __builtin_amdgcn_permlane16_swap(x1, y1, false, false);
				st16(rs, u32x4{r0[0], r1v[0], r0[1], r1v[1]}, v_f, w1f_o + u * 28 * 1024);
			}
		}
		check_flag(rs, fa, true, tid * 128, O_FLAGA, tag);
		LBAR();
		st8(rs, tag, (b | tid) ? OOB : 0u, O_EPOCH);
		STAMP(5);
		{
			p0_gemm<-(int)L_TOP>(sm + L_TOP, qf, acc, g, i16);
			const int kk = 4 * pf + g;
			const unsigned kv = fold ? (28 * ((kk >> 1) & 3) + 2 * (kk >> 3) + (kk & 1)) * 1024 + (16 * (w & 1) + i16) * 16 : OOB;
#pragma unroll
			for (int hh = 0; hh < 2; ++hh) {
				u32x4 o = {pack2(acc[hh][0] * scj, acc[hh][1] * scj), pack2(acc[hh][2] * scj, acc[hh][3] * scj),
						   pack2(acc[2 + hh][0] * scj, acc[2 + hh][1] * scj), pack2(acc[2 + hh][2] * scj, acc[2 + hh][3] * scj)};
				st16(rs, o, kv + hh * 512, O_W2F + (2 * jt + (w >> 1)) * 112 * 1024);
			}
			const int kw = 4 * pf + w;
			const unsigned ko = (28 * ((kw >> 1) & 3) + 2 * (kw >> 3) + (kw & 1)) * 1024;
			const unsigned sg = 2 * w + ((q4 >> 1) & 1);
#pragma unroll
			for (int u = 0; u < 2; ++u) {
				unsigned ch = 8 * jt + ((4 * u + 2 * (g & 1) + (pc >> 1)) ^ sg);
				const u8* p = sm + (8 * w + 4 * h + q4) * 896 + ch * 16 + (pc & 1) * 8;
				st16(rs, cat(trread(p + L_TOP), trread(p)), v_f, O_W2O + (2 * jt + u) * 112 * 1024 + ko);
			}
			if (p0 == 28) {
				f32x4 c = {0.f, 0.f, 0.f, 0.f};
#pragma unroll
				for (int ks = 0; ks < 14; ++ks) c = mfma16(*(const u32x4*)(sm + L_TOP + 28672 + 64 * ks + 16 * g), qf[ks], c);
				const float c0 = c[0];
				st4(rs, __builtin_bit_cast(unsigned, c0 * scj), g ? OOB : i16 * 4, O_B2 + (64 * jt + 16 * w) * 4);
			}
		}
		STAMP(6);
	}
	u32x4 wv[2];
	auto layer = [&](auto LC) __attribute__((always_inline)) {
		constexpr int l = decltype(LC)::value;
		int tid = threadIdx.x;
		PIN(tid);
		const int lane = tid & 63, g = lane >> 4, i16 = lane & 15, h = lane >> 5, q4 = (lane >> 2) & 3, pc = lane & 3;
		const unsigned o_ci = l ? O_XT2 : O_XT0, o_co = l ? O_XT3 : O_XT1, o_h = l ? O_H2 : O_H1;
		const unsigned o_w[2] = {(l ? O_W2O : O_W2F) + ((2 * jt + (w & 1)) * 112 + 7 * w) * 1024, (l ? O_W2O : O_W2F) + ((2 * jt + ((w & 1) ^ 1)) * 112 + 7 * w) * 1024};
		const unsigned o_hl[2] = {o_h + ((2 * pt + (w >> 1)) * 112 + 7 * w) * 1024, o_h + ((2 * pt + ((w >> 1) ^ 1)) * 112 + 7 * w) * 1024};
		const unsigned l16 = lane * 16;
		u32x4 W[28][2], H[28][2];
		u32x4 w1s[28];
		u32x2 fb[4];
#define LDF(arr, base, q) arr[(q) >> 1][(q) & 1] = ld16(rs, l16 + ((((q) >> 1) % 7) & 3) * 1024, base[(q) & 1] + ((((q) >> 1) / 7) * 28 + ((((q) >> 1) % 7) & 4)) * 1024)
#define LDW(q) LDF(W, o_w, q)
#define LDH(q) LDF(H, o_hl, q)
#define LDW1(n) w1s[n] = ld16(rs, l16 + ((n) & 3) * 1024, O_W1F + ((4 * ft + w) * 28 + ((n) & ~3)) * 1024)
#define HOOK(k) do { SB(); if (l == 0) { LDW1(2 * (k)); LDW1(2 * (k) + 1); } else { LDW(2 * (k)); LDW(2 * (k) + 1); } SB(); } while (0)
		{
			STAMP(l ? 16 : 7);
			const unsigned xo = (16 * w + ((lane & 31) >> 2)) * STRIP + h * 64 + pc * 16;
			const unsigned wo = (16 * w + i16) * 128 + g * 16;
			u32x4 xr, xi, ssp[7];
			unsigned m;
#define COL_LOAD() do { \
				xr = ld16(rs, xo, o_ci + b * 128); xi = ld16(rs, xo, o_ci + b * 128 + 8 * STRIP); \
				if (l == 0) { \
					ssp[0] = ld16(rs, tid * 16, O_SS0); \
					wv[0] = ld16(rs, wo, O_WT + b * 8192); wv[1] = ld16(rs, wo + 64, O_WT + b * 8192); \
				} else { \
					_Pragma("unroll") for (int k = 0; k < 7; ++k) ssp[k] = ld16(rs, tid * 16, O_SS1 + k * 4096); \
				} } while (0)
#define COL_MAX() do { \
				m = mx4(mx4(0, xr), xi); \
				if (l == 0) m = mx4(mx4(mx4(m, ssp[0]), wv[0]), wv[1]); \
				else { _Pragma("unroll") for (int k = 0; k < 7; ++k) m = mx4(m, ssp[k]); } } while (0)
			COL_LOAD();
			if (l == 0) LBAR();
			COL_MAX();
			if (UNLIKELY(anyp(m))) {
#pragma unroll 1
				for (int it = 0; it < SPIN; ++it) {
					__builtin_amdgcn_s_sleep(2);
					FENCE();
					CNT(48 + l);
					if (m == POIS) { COL_LOAD(); COL_MAX(); }
					if (!anyp(m)) break;
				}
			}
			STAMP(l ? 26 : 30);
			swap32(xr, xi);
			float fr[8], fi[8];
			u8* cs = sm + L_TOP;
			{
				float rr[8];
#pragma unroll
				for (int i = 0; i < 4; ++i) {
					float lo = bflo(ssp[0][i]), hi = bfhi(ssp[0][i]);
					if (l == 1) {
#pragma unroll
						for (int k = 1; k < 7; ++k) { lo = dot2(ssp[k][i], 0x00003f80u, lo); hi = dot2(ssp[k][i], 0x3f800000u, hi); }
					}
					rr[2 * i] = __builtin_amdgcn_rsqf(__builtin_fmaf(lo, 1.0f / 448.0f, EPS));
					rr[2 * i + 1] = __builtin_amdgcn_rsqf(__builtin_fmaf(hi, 1.0f / 448.0f, EPS));
					fr[2 * i] = bflo(xr[i]); fr[2 * i + 1] = bfhi(xr[i]);
					fi[2 * i] = bflo(xi[i]); fi[2 * i + 1] = bfhi(xi[i]);
				}
				const unsigned imask = b ? 0xffffffffu : 0u;
				u8* p = cs + (tid >> 3) * 160 + (tid & 7) * 16;
				*(u32x4*)p = u32x4{pack2(fr[0] * rr[0], fr[1] * rr[1]), pack2(fr[2] * rr[2], fr[3] * rr[3]), pack2(fr[4] * rr[4], fr[5] * rr[5]), pack2(fr[6] * rr[6], fr[7] * rr[7])};
				*(u32x4*)(p + 5120) = u32x4{pack2(fi[0] * rr[0], fi[1] * rr[1]) & imask, pack2(fi[2] * rr[2], fi[3] * rr[3]) & imask,
											pack2(fi[4] * rr[4], fi[5] * rr[5]) & imask, pack2(fi[6] * rr[6], fi[7] * rr[7]) & imask};
			}
			HOOK(0);
			WAIT_BAR(2);
			if (l == 0) st8(rs, tag, tid ? OOB : 0u, O_FLAGB + b * 128);
			STAMP(l ? 17 : 8);
			const f32x4 z4 = {0.f, 0.f, 0.f, 0.f};
			const u32x4 sgn = {0x80008000u, 0x80008000u, 0x80008000u, 0x80008000u};
			const u32x4 N1[2] = {K.S1[0] ^ sgn, K.S1[1] ^ sgn}, N2v[2] = {K.S2[0] ^ sgn, K.S2[1] ^ sgn};
			f32x4 are[2], aim[2];
			u32x4 Bre, Bim;
			u32x4 mr[4], mi[4];
#pragma unroll
			for (int q = 0; q < 4; ++q) {
				const u8* p = cs + (16 * (q >> 1) + i16) * 160 + 64 * (q & 1) + 16 * g;
				mr[q] = *(const u32x4*)p; mi[q] = *(const u32x4*)(p + 5120);
			}
			HOOK(1);
#pragma unroll
			for (int ks = 0; ks < 2; ++ks) {
#pragma unroll
				for (int t = 0; t < 2; ++t) {
					are[t] = mfma16(mr[2 * t + ks], K.C1[ks], ks ? are[t] : z4);
					aim[t] = mfma16(mi[2 * t + ks], K.C1[ks], ks ? aim[t] : z4);
				}
#pragma unroll
				for (int t = 0; t < 2; ++t) {
					are[t] = mfma16(mi[2 * t + ks], K.S1[ks], are[t]);
					aim[t] = mfma16(mr[2 * t + ks], N1[ks], aim[t]);
				}
				PINA(are[0]); PINA(are[1]); PINA(aim[0]); PINA(aim[1]);
				HOOK(2 + ks);
			}
#pragma unroll
			for (int t = 0; t < 2; ++t)
#pragma unroll
				for (int e = 0; e < 4; e += 2) {
					int i = 4 * t + e;
					Bre[i >> 1] = pack2(are[t][e] * K.twc[i] + aim[t][e] * K.tws[i], are[t][e + 1] * K.twc[i + 1] + aim[t][e + 1] * K.tws[i + 1]);
					Bim[i >> 1] = pack2(aim[t][e] * K.twc[i] - are[t][e] * K.tws[i], aim[t][e + 1] * K.twc[i + 1] - are[t][e + 1] * K.tws[i + 1]);
				}
			PIN(Bre); PIN(Bim);
			HOOK(4);
#pragma unroll
			for (int t = 0; t < 2; ++t) {
				are[t] = mfma16(K.C2[t], Bre, z4);
				aim[t] = mfma16(K.C2[t], Bim, z4);
			}
#pragma unroll
			for (int t = 0; t < 2; ++t) {
				are[t] = mfma16(K.S2[t], Bim, are[t]);
				aim[t] = mfma16(N2v[t], Bre, aim[t]);
			}
			PINA(are[0]); PINA(are[1]); PINA(aim[0]); PINA(aim[1]);
			HOOK(5);
#pragma unroll
			for (int t = 0; t < 2; ++t)
#pragma unroll
				for (int e = 0; e < 4; e += 2) {
					float a0 = bflo(wv[t][e]), b0 = bfhi(wv[t][e]), a1 = bflo(wv[t][e + 1]), b1v = bfhi(wv[t][e + 1]);
					Bre[2 * t + (e >> 1)] = pack2(are[t][e] * a0 - aim[t][e] * b0, are[t][e + 1] * a1 - aim[t][e + 1] * b1v);
					Bim[2 * t + (e >> 1)] = pack2(are[t][e] * b0 + aim[t][e] * a0, are[t][e + 1] * b1v + aim[t][e + 1] * a1);
				}
			PIN(Bre); PIN(Bim);
			HOOK(6);
#pragma unroll
			for (int t = 0; t < 2; ++t) {
				are[t] = mfma16(Bre, K.C2[t], z4);
				aim[t] = mfma16(Bim, K.C2[t], z4);
			}
#pragma unroll
			for (int t = 0; t < 2; ++t) {
				are[t] = mfma16(Bim, N2v[t], are[t]);
				aim[t] = mfma16(Bre, K.S2[t], aim[t]);
			}
			PINA(are[0]); PINA(are[1]); PINA(aim[0]); PINA(aim[1]);
			HOOK(7);
#pragma unroll
			for (int t = 0; t < 2; ++t) {
				float vr[4], vi[4];
#pragma unroll
				for (int e = 0; e < 4; ++e) {
					vr[e] = are[t][e] * K.tic[4 * t + e] - aim[t][e] * K.tis[4 * t + e];
					vi[e] = aim[t][e] * K.tic[4 * t + e] + are[t][e] * K.tis[4 * t + e];
				}
				u8* p = cs + 10240 + (16 * t + i16) * 160 + 32 * w + 8 * g;
				*(u32x2*)p = u32x2{pack2(vr[0], vr[1]), pack2(vr[2], vr[3])};
				*(u32x2*)(p + 5120) = u32x2{pack2(vi[0], vi[1]), pack2(vi[2], vi[3])};
			}
			HOOK(8);
			LBAR();
#pragma unroll
			for (int q = 0; q < 4; ++q) {
				const u8* p = cs + 10240 + (16 * (q >> 1) + i16) * 160 + 64 * (q & 1) + 16 * g;
				mr[q] = *(const u32x4*)p; mi[q] = *(const u32x4*)(p + 5120);
			}
			HOOK(9);
#pragma unroll
			for (int ks = 0; ks < 2; ++ks) {
#pragma unroll
				for (int t = 0; t < 2; ++t) {
					are[t] = mfma16(K.C1[ks], mr[2 * t + ks], ks ? are[t] : z4);
					aim[t] = mfma16(K.C1[ks], mi[2 * t + ks], ks ? aim[t] : z4);
				}
#pragma unroll
				for (int t = 0; t < 2; ++t) {
					are[t] = mfma16(N1[ks], mi[2 * t + ks], are[t]);
					aim[t] = mfma16(K.S1[ks], mr[2 * t + ks], aim[t]);
				}
				PINA(are[0]); PINA(are[1]); PINA(aim[0]); PINA(aim[1]);
				HOOK(10 + ks);
			}
#pragma unroll
			for (int t = 0; t < 2; ++t) {
				u8* p = cs + 20480 + (16 * t + i16) * 272 + 64 * w + 16 * g;
				*(f32x4*)p = are[t];
				*(f32x4*)(p + 8704) = aim[t];
			}
			HOOK(12);
			LBAR();
			{
				const u8* p = cs + 20480 + (tid >> 3) * 272 + (tid & 7) * 32;
				f32x4 r0 = *(const f32x4*)p, r1 = *(const f32x4*)(p + 16), i0 = *(const f32x4*)(p + 8704), i1 = *(const f32x4*)(p + 8720);
				const float im = b ? 1.0f : 0.0f;
				xr = u32x4{pack2c(fr[0] + r0[0], fr[1] + r0[1]), pack2c(fr[2] + r0[2], fr[3] + r0[3]), pack2c(fr[4] + r1[0], fr[5] + r1[1]), pack2c(fr[6] + r1[2], fr[7] + r1[3])};
				xi = u32x4{pack2c(fi[0] + im * i0[0], fi[1] + im * i0[1]), pack2c(fi[2] + im * i0[2], fi[3] + im * i0[3]),
						   pack2c(fi[4] + im * i1[0], fi[5] + im * i1[1]), pack2c(fi[6] + im * i1[2], fi[7] + im * i1[3])};
				swap32(xr, xi);
				st16(rs, xr, xo, o_co + b * 128);
				st16(rs, xi, xo, o_co + b * 128 + 8 * STRIP);
			}
			HOOK(13);
			STAMP(l ? 18 : 9);
		}
		if (tid < 32) *(f32x4*)(sm + L_TAB + tid * 16) = f32x4{bflo(b1r[0]), bfhi(b1r[0]), bflo(b1r[1]), bfhi(b1r[1])};
		if (l == 0) {
			const unsigned fo = lane < 56 ? lane * 512 : OOB;
#pragma unroll
			for (int k = 0; k < 4; ++k) fb[k] = ld8(rs, fo + k * 128, O_FLAGB);
			SB();
			unsigned m = 0;
#pragma unroll
			for (int n = 0; n < 28; ++n) m = mx4(m, w1s[n]);
			if (UNLIKELY(anyp(m))) {
#pragma unroll 1
				for (int it = 0; it < SPIN; ++it) {
					__builtin_amdgcn_s_sleep(2);
					FENCE();
					m = 0; CNT(47);
#pragma unroll
					for (int n = 0; n < 28; ++n) { LDW1(n); m = mx4(m, w1s[n]); }
					if (!anyp(m)) break;
				}
			}
#pragma unroll
			for (int n = 0; n < 8; ++n) *(u32x4*)(sm + (w * 28 + n) * 1024 + l16) = w1s[n];
			SB();
#pragma unroll
			for (int k = 0; k < 4; ++k) check_flag(rs, fb[k], lane < 56, fo + k * 128, O_FLAGB, tag);
			STAMP(31);
#pragma unroll
			for (int q = 0; q < 28; ++q) {
				LDW(q);
				if (q < 20) *(u32x4*)(sm + (w * 28 + 8 + q) * 1024 + l16) = w1s[8 + q];
				SB();
			}
		} else {
#pragma unroll
			for (int q = 28; q < 56; ++q) LDW(q);
			SB();
		}
		STAMP(l ? 19 : 10);
		const unsigned o_xs = o_co + (4 * pp + w) * STRIP;
		{
			bool ok = lane >= 56;
#pragma unroll 1
			for (int it = 0; it < SPIN; ++it) {
				CNT(50 + l);
				if (!ok) ok = ld4(rs, (56 * w + lane) * 128 + 124, o_xs) != POIS;
				if (__builtin_amdgcn_ballot_w64(!ok) == 0) break;
				__builtin_amdgcn_s_sleep(2);
				FENCE();
			}
			LBAR();
		}
		STAMP(l ? 20 : 11);
		{
			u32x4 xs[28], B[28];
#define LDX(n) xs[n] = ld16(rs, l16 + ((n) & 3) * 1024, o_xs + ((n) & ~3) * 1024)
#pragma unroll
			for (int n = 0; n < 12; ++n) { LDX(n); SB(); }
			f32x16 ac[4];
			f32x4 gr[2];
			f32x16 z16;
#pragma unroll
			for (int r = 0; r < 16; ++r) z16[r] = 0.f;
			const f32x4 z4 = {0.f, 0.f, 0.f, 0.f};
			unsigned o_wp = L_TOP + w * 8192 + l16, o_tp = L_TOP + w * 8192 + (8 * h + q4) * 64 + (g & 1) * 32 + (pc & 1) * 16 + (pc >> 1) * 8;
			unsigned o_a1 = l16 + 57344, o_tab = L_TAB + h * 16;
			PIN(o_wp); PIN(o_tp); PIN(o_a1); PIN(o_tab);
			u8* wp = sm + o_wp;
			const u8* tp = sm + o_tp;
			const u8* ap[2] = {sm + l16, sm + o_a1};
#define LDA(q) A[(q) % NA] = *(const u32x4*)(ap[(q) / 56] + ((q) % 56) * 1024)
			constexpr int NA = 8, DA = 5;
			u32x4 A[NA];
			u32x2 tlo[4];
			float r2;
#pragma unroll
			for (int c = 0; c < 8; ++c) {
				if (c < 7) {
					unsigned m = 0;
#pragma unroll
					for (int k = 0; k < 4; ++k) m = mx4(m, xs[4 * c + k]);
					if (UNLIKELY(anyp(m))) {
#pragma unroll 1
						for (int it = 0; it < SPIN; ++it) {
							__builtin_amdgcn_s_sleep(2);
							FENCE();
							m = 0; CNT(l * 8 + c);
#pragma unroll
							for (int k = 0; k < 4; ++k) { LDX(4 * c + k); m = mx4(m, xs[4 * c + k]); }
							if (!anyp(m)) break;
						}
					}
				}
				SB();
				if (c == 0 || c == 2 || c == 4 || c == 6) STAMP((l ? 42 : 32) + c / 2);
#pragma unroll
				for (int i = 0; i < 8; ++i) {
					if (c > 0) {
						const int k = (c < 7) ? (i >> 1) : (i & 3), ks = 4 * (c - 1) + k;
						if ((c < 7) ? (i & 1) : (i >> 2)) {
							ac[0] = mfma32(A[ks % NA], B[ks], ks ? ac[0] : z16);
							PINA(ac[0]);
						} else {
							u32x2 m0 = {B[ks][0], B[ks][1]};
							if (ks == 0) m0[0] &= h ? 0xffffffffu : 0x0000ffffu;
							gr[0] = mfma4(m0, u32x2{B[ks][0], B[ks][1]}, ks ? gr[0] : z4);
							gr[1] = mfma4(u32x2{B[ks][2], B[ks][3]}, u32x2{B[ks][2], B[ks][3]}, ks ? gr[1] : z4);
							PINA(gr[0]); PINA(gr[1]);
						}
					}
					if (c < 7) {
#pragma unroll
						for (int o = 2 * i; o < 2 * i + 2; ++o) {
							if (o < 4) *(u32x4*)(wp + ((4 * c + o) & 7) * 1024) = xs[4 * c + o];
							else if (o < 12) {
								const int k = (o - 4) >> 1, ks = 4 * c + k;
								if (o & 1) B[ks] = cat(tlo[k], trread(tp + (ks & 7) * 1024 + 256));
								else tlo[k] = trread(tp + (ks & 7) * 1024);
							} else {
								LDA(4 * c + o - 12);
							}
						}
						if (i & 1) {
							const int n = 4 * c + 12 + (i >> 1);
							if (n < 28) LDX(n);
							else if (l == 0) LDW(n);
						}
					} else {
						if (i >= 4 && i - 4 < DA - 1) LDA(28 + i - 4);
						if (i == 6) {
							f32x4 s4 = gr[0] + gr[1];
							float sa = (lane & 1) ? s4[1] : s4[0], sb = (lane & 1) ? s4[3] : s4[2];
							int si = __builtin_bit_cast(int, (lane & 2) ? sb : sa);
							auto sw = __builtin_amdgcn_permlane32_swap(si, si, false, false);
							const unsigned s0 = sw[0], s1 = sw[1];
							float v = (__builtin_bit_cast(float, s0) + __builtin_bit_cast(float, s1)) * (1.0f / 448.0f);
							float r1 = __builtin_amdgcn_rsqf(v + EPS);
							r2 = r1 * __builtin_amdgcn_rsqf(__builtin_fmaf(v * r1, r1, EPS));
							PIN(r2);
						}
					}
					SB();
				}
			}
			STAMP(l ? 21 : 12);
			const u8* tab = sm + o_tab;
			const unsigned o_hs = o_h + ((4 * pp + w) * 112 + 2 * ft) * 1024;
			GS gs;
#pragma unroll
			for (int st = 1; st < 5; ++st) {
#pragma unroll
				for (int q = 0; q < 4; ++q) gs.bb[q] = *(const f32x4*)(tab + (st - 1) * 128 + q * 32);
#pragma unroll
				for (int i = 0; i < 28; ++i) {
					const int q = 28 * st + i;
					if (st < 4) {
						ac[st] = mfma32(A[q % NA], B[i], i ? ac[st] : z16);
						PINA(ac[st]);
						if (q + DA - 1 < 112) LDA(q + DA - 1);
					}
					if (i >= 2 && i < 27) gelu_blk(gs, i - 2, ac[st - 1], r2);
					if (i == 14) st16(rs, u32x4{gs.o[0], gs.o[1], gs.o[2], gs.o[3]}, l16, o_hs + (st - 1) * 28 * 1024);
					if (i == 26) st16(rs, u32x4{gs.o[4], gs.o[5], gs.o[6], gs.o[7]}, l16 + 1024, o_hs + (st - 1) * 28 * 1024);
					if (l == 0 && st < 3 && (i & 1) && i < 16) LDW(40 + 8 * (st - 1) + (i >> 1));
					if (st == 3 && i >= HS0 && ((i - HS0) & 1) == 0 && (i - HS0) / 2 < 14) LDH((i - HS0) / 2);
					if (st == 4 && (i % 3) == 0 && (28 - HS0 + 1) / 2 + i / 3 < 14) LDH((28 - HS0 + 1) / 2 + i / 3);
					SB();
				}
				STAMP((l ? 46 : 36) + st - 1);
			}
		}
		LBAR();
		STAMP(l ? 23 : 14);
		{
			f32x16 acc[4], z16;
#pragma unroll
			for (int r = 0; r < 16; ++r) z16[r] = 0.f;
			u32x4 res[2], xq[14], qb[14];
			unsigned bias = 0;
			u8* ex = sm + L_TOP;
			u8* wq = sm + w * 16384 + l16;
			const u8* tq = sm + w * 16384 + (8 * h + q4) * 64 + (g & 1) * 32 + (pc & 1) * 16 + (pc >> 1) * 8;
			unsigned m;
#define H_CHECK(n0, n1) do { \
				m = 0; \
				_Pragma("unroll") for (int k = (n0); k < (n1); ++k) m = mx4(mx4(m, H[k][0]), H[k][1]); \
			} while (0)
#define H_RETRY(n0, n1) \
			if (UNLIKELY(anyp(m))) { \
				_Pragma("unroll 1") for (int it = 0; it < SPIN; ++it) { \
					__builtin_amdgcn_s_sleep(2); \
					FENCE(); \
					CNT(16 + l * 16 + (n0) / 2); \
					_Pragma("unroll") for (int k = 2 * (n0); k < 2 * (n1); ++k) LDH(k); \
					H_CHECK(n0, n1); \
					if (!anyp(m)) break; \
				} \
			}
			H_CHECK(0, 1);
			H_RETRY(0, 1)
			SB();
#pragma unroll
			for (int n = 0; n < 21; ++n) {
				acc[0] = mfma32(H[n][0], W[n][0], n ? acc[0] : z16);
				PINA(acc[0]);
				LDH(2 * n + 14);
				SB();
				acc[1] = mfma32(H[n][0], W[n][1], n ? acc[1] : z16);
				PINA(acc[1]);
				if (n < 20) H_CHECK(n + 1, n + 2);
				SB();
				acc[2] = mfma32(H[n][1], W[n][0], n ? acc[2] : z16);
				PINA(acc[2]);
				LDH(2 * n + 15);
				SB();
				acc[3] = mfma32(H[n][1], W[n][1], n ? acc[3] : z16);
				PINA(acc[3]);
				if (l == 0) {
					if (n == 8) bias = ld4(rs, (lane & 31) * 4, O_B2 + (64 * jt + 32 * (w & 1)) * 4);
					if (n == 9 || n == 10) res[n - 9] = ld16(rs, (lane & 31) * 64 + h * 16 + (n - 9) * 32, O_XT1 + (2 * pt + (w >> 1)) * STRIP + (64 * jt + 32 * (w & 1)) * 64);
				} else {
					if (n < 7) {
						xq[n] = ld16(rs, l16, O_XT3 + (2 * pt + (w >> 1)) * STRIP + (7 * w + n) * 1024);
					} else if (n < 14) {
						xq[n] = ld16(rs, l16, O_XT3 + (2 * pt + ((w >> 1) ^ 1)) * STRIP + (7 * w + n - 7) * 1024);
						*(u32x4*)(wq + (n - 7) * 1024) = xq[n - 7];
						qb[n - 7] = ld16(rs, l16, O_QT + ((2 * jt + (w & 1)) * 28 + 7 * w + n - 7) * 1024);
					} else {
						*(u32x4*)(wq + (n - 7) * 1024) = xq[n - 7];
						qb[n - 7] = ld16(rs, l16, O_QT + ((2 * jt + ((w & 1) ^ 1)) * 28 + 7 * w + n - 14) * 1024);
						xq[n - 14] = cat(trread(tq + (n - 14) * 1024), trread(tq + (n - 14) * 1024 + 256));
					}
					if (n == 6) bias = (unsigned)*(const u16*)(b2 + (64 * jt + 32 * (w & 1) + (lane & 31)) * 2) << 16;
				}
				SB();
				if (n < 20) { H_RETRY(n + 1, n + 2) }
			}
			STAMP(l ? 50 : 40);
			if (l == 1) {
#pragma unroll
				for (int i = 0; i < 14; ++i) {
					acc[2 * (i / 7)] = mfma32(xq[i], qb[i % 7], acc[2 * (i / 7)]);
					PINA(acc[2 * (i / 7)]);
					if (i < 7) xq[7 + i] = cat(trread(tq + (7 + i) * 1024), trread(tq + (7 + i) * 1024 + 256));
					SB();
					acc[2 * (i / 7) + 1] = mfma32(xq[i], qb[7 + i % 7], acc[2 * (i / 7) + 1]);
					PINA(acc[2 * (i / 7) + 1]);
					SB();
				}
			}
			H_CHECK(21, 28);
			H_RETRY(21, 28)
			SB();
			STAMP(l ? 51 : 41);
#pragma unroll
			for (int a = 1; a < 5; ++a) {
#pragma unroll
				for (int n = 21; n < 28; ++n) {
					acc[a & 3] = mfma32(H[n][(a >> 1) & 1], W[n][a & 1], acc[a & 3]);
					PINA(acc[a & 3]);
					if (a > 1 && n >= 23 && n < 27) {
						const int q = n - 23, i = a - 1;
						*(f32x4*)(ex + (w ^ i) * 12288 + (i - 1) * 4096 + l16 + q * 1024) = f32x4{acc[i][4 * q], acc[i][4 * q + 1], acc[i][4 * q + 2], acc[i][4 * q + 3]};
					}
					SB();
				}
			}
			STAMP(l ? 24 : 15);
			LBAR();
			float v[16];
			const float bf = __builtin_bit_cast(float, bias);
#pragma unroll
			for (int q = 0; q < 4; ++q) {
				const u8* p = ex + w * 12288 + q * 1024 + l16;
				f32x4 s = (*(const f32x4*)p + *(const f32x4*)(p + 4096)) + *(const f32x4*)(p + 8192);
#pragma unroll
				for (int e = 0; e < 4; ++e) {
					v[4 * q + e] = (acc[0][4 * q + e] + bf) + s[e];
					if (l == 0) v[4 * q + e] += (e & 1) ? bfhi(res[q >> 1][2 * (q & 1) + (e >> 1)]) : bflo(res[q >> 1][2 * (q & 1) + (e >> 1)]);
				}
			}
			u32x4 X = {pack2c(v[0], v[1]), pack2c(v[2], v[3]), pack2c(v[4], v[5]), pack2c(v[6], v[7])};
			u32x4 Y = {pack2c(v[8], v[9]), pack2c(v[10], v[11]), pack2c(v[12], v[13]), pack2c(v[14], v[15])};
			if (l == 0) {
				swap16(X, Y);
				const unsigned o = O_XT2 + (2 * pt + (w >> 1)) * STRIP + (64 * jt + 32 * (w & 1)) * 64;
				const unsigned vo = i16 * 64 + (g & 1) * 32 + h * 16;
				st16(rs, X, vo, o);
				st16(rs, Y, vo + 1024, o);
				SB();
				const float keep = (64 * jt + 32 * (w & 1) + (lane & 31) == 1) ? 0.f : 1.f;
				float s[8];
#pragma unroll
				for (int i = 0; i < 8; ++i) {
					int a = __builtin_bit_cast(int, v[i] * v[i] * keep), c = __builtin_bit_cast(int, v[i + 8] * v[i + 8] * keep);
					auto r = __builtin_amdgcn_permlane16_swap(a, c, false, false);
					const unsigned r0 = r[0], r1 = r[1];
					s[i] = __builtin_bit_cast(float, r0) + __builtin_bit_cast(float, r1);
				}
#pragma unroll
				for (int i = 0; i < 4; ++i) s[i] = ((lane & 8) ? s[i + 4] : s[i]) + dpp<0x128>((lane & 8) ? s[i] : s[i + 4]);
#pragma unroll
				for (int i = 0; i < 2; ++i) s[i] = ((lane & 4) ? s[i + 2] : s[i]) + dpp<0x141>((lane & 4) ? s[i] : s[i + 2]);
				s[0] = ((lane & 2) ? s[1] : s[0]) + dpp<0x4e>((lane & 2) ? s[0] : s[1]);
				s[0] += dpp<0xb1>(s[0]);
				if (!(lane & 1)) *(float*)(ex + w * 12288 + 11264 + (16 * (g & 1) + 8 * h + ((lane >> 1) & 7)) * 4) = s[0];
				LBAR();
				if (tid < 8) {
					const u8* p = ex + (tid >> 2) * 24576 + 11264 + (tid & 3) * 32;
					f32x4 s0 = *(const f32x4*)p + *(const f32x4*)(p + 12288), s1 = *(const f32x4*)(p + 16) + *(const f32x4*)(p + 12304);
					st16(rs, u32x4{pack2c(s0[0], s0[1]), pack2c(s0[2], s0[3]), pack2c(s1[0], s1[1]), pack2c(s1[2], s1[3])}, tid * 16, O_SS1 + jt * 4096 + pt * 128);
				}
				const u32x2 fp = ld8(rs, lane < 32 ? lane * 128 : OOB, O_FLAGP);
				SB();
				STAMP(13);
				int ln = lane;
				PIN(ln);
#pragma unroll
				for (int part = 0; part < 24; ++part) col_part(K, part, w, ln >> 4, ln & 15);
				check_flag(rs, fp, lane < 32, lane * 128, O_FLAGP, tag);
			} else {
				u8* im = ex + w * 12288;
				*(u32x4*)(im + (lane & 31) * 64 + h * 16) = X;
				*(u32x4*)(im + (lane & 31) * 64 + h * 16 + 32) = Y;
#pragma unroll
				for (int u = 0; u < 2; ++u) {
					const u8* p = im + (8 * g + q4) * 64 + 32 * u + 8 * pc;
					*(u32x4*)(out + (32 * (32 * (w >> 1) + 16 * u + i16) + pt) * 896 + (64 * jt + 32 * (w & 1) + 8 * g) * 2) = cat(trread(p), trread(p + 256));
				}
			}
		}
	};
	layer(IC<0>{});
	layer(IC<1>{});
	STAMP(25);
	FLUSH();
}

extern "C" void kernel_launch(void* const* d_in, const int* in_sizes, int n_in, void* d_out, int out_size, void* d_ws, size_t ws_size,
		hipStream_t stream) {
	k_all<<<256, 256, 0, stream>>>((const u8*)d_in[0], (const u8*)d_in[1], (const u8*)d_in[2], (const u8*)d_in[4], (const u8*)d_in[5],
		(const u8*)d_in[6], (const u8*)d_in[7], (u8*)d_out, (u8*)d_ws);
}
```

```cpp
#include <hip/hip_runtime.h>
#include <hip/hip_bf16.h>
#include <stdint.h>

typedef __bf16 bf16x8 __attribute__((ext_vector_type(8)));
typedef __bf16 bf16x2 __attribute__((ext_vector_type(2)));
typedef short s16x4 __attribute__((ext_vector_type(4)));
typedef float f32x2 __attribute__((ext_vector_type(2)));
typedef float f32x4 __attribute__((ext_vector_type(4)));
typedef float f32x16 __attribute__((ext_vector_type(16)));
typedef unsigned u32x2 __attribute__((ext_vector_type(2)));
typedef unsigned u32x4 __attribute__((ext_vector_type(4)));
typedef u32x4 u32x4u __attribute__((aligned(2)));
typedef unsigned char u8;
typedef unsigned short u16;
typedef __amdgpu_buffer_rsrc_t rsrc_t;
template <int V> struct IC { static constexpr int value = V; };
#define LAS __attribute__((address_space(3)))
#define DEV __device__ __forceinline__

#define PADS 128
#define PADW 128
constexpr unsigned STRIP = 28672u + PADS;
constexpr unsigned WTS = 8192u + PADW;
constexpr unsigned XT_B = 64u * STRIP;
constexpr unsigned FR_B = 1605632u;
constexpr unsigned H_B = 7340032u;
#define FS 8
constexpr unsigned O_FLAGA = 0, O_FLAGB = 16384, O_FLAGC = 32768, O_FLAGD = 49152, O_EPOCH = 65536;
constexpr unsigned O_B2 = 66560;
constexpr unsigned O_SS0 = 70656, O_SS1 = 74752;
constexpr unsigned O_XT0 = 131072;
constexpr unsigned O_XT1 = O_XT0 + XT_B;
constexpr unsigned O_XT2 = O_XT1 + XT_B;
constexpr unsigned O_H1 = O_XT2 + XT_B;
constexpr unsigned O_XT3 = O_H1 + H_B;
constexpr unsigned O_H2 = O_XT3 + XT_B;
constexpr unsigned O_W1F = O_H2 + H_B;
constexpr unsigned O_W2F = O_W1F + FR_B;
constexpr unsigned O_W2O = O_W2F + FR_B;
constexpr unsigned O_QT = O_W2O + FR_B;
constexpr unsigned O_WT = O_QT + 401408;
constexpr unsigned WS_END = O_WT + 224 * WTS;
constexpr unsigned L_TOP = 114688;
constexpr unsigned L_TAB = L_TOP + 48640;

constexpr float EPS = 1e-5f;
constexpr float SC1 = 0.04724555912615340f;
constexpr float SC2 = 0.06681531047810609f;
constexpr unsigned POIS = 0xffffffffu;
constexpr unsigned OOB = 0x80000000u;
#define SPINB 18
constexpr int SPIN = 1 << SPINB;

DEV unsigned pack2(float a, float b) {
	f32x2 v = {a, b};
	return __builtin_bit_cast(unsigned, __builtin_convertvector(v, bf16x2));
}
DEV unsigned pack2c(float a, float b) { return min(pack2(a, b), 0xfffffffeu); }
DEV float bflo(unsigned u) { return __builtin_bit_cast(float, u << 16); }
DEV float bfhi(unsigned u) { return __builtin_bit_cast(float, u & 0xffff0000u); }
DEV f32x4 mfma16(u32x4 a, u32x4 b, f32x4 c) {
	return __builtin_amdgcn_mfma_f32_16x16x32_bf16(__builtin_bit_cast(bf16x8, a), __builtin_bit_cast(bf16x8, b), c, 0, 0, 0);
}
DEV f32x16 mfma32(u32x4 a, u32x4 b, f32x16 c) {
	return __builtin_amdgcn_mfma_f32_32x32x16_bf16(__builtin_bit_cast(bf16x8, a), __builtin_bit_cast(bf16x8, b), c, 0, 0, 0);
}
DEV f32x4 mfma4(u32x2 a, u32x2 b, f32x4 c) {
	return __builtin_amdgcn_mfma_f32_4x4x4bf16_1k(__builtin_bit_cast(s16x4, a), __builtin_bit_cast(s16x4, b), c, 0, 0, 0);
}
DEV rsrc_t mkrs(const void* p, unsigned bytes) { return __builtin_amdgcn_make_buffer_rsrc((void*)p, 0, bytes, 0x00020000); }
DEV void dma16(rsrc_t rs, u8* lds, unsigned voff, unsigned soff) {
	__builtin_amdgcn_raw_ptr_buffer_load_lds(rs, (LAS void*)lds, 16, voff, soff, 0, 0);
}
template <int OFF> DEV void dma16c(rsrc_t rs, u8* lds, unsigned voff, unsigned soff) {
	__builtin_amdgcn_raw_ptr_buffer_load_lds(rs, (LAS void*)lds, 16, voff, soff, OFF, 16);
}
DEV u32x4 ld16(rsrc_t rs, unsigned v, unsigned s) { return __builtin_amdgcn_raw_buffer_load_b128(rs, v, s, 16); }
DEV u32x2 ld8(rsrc_t rs, unsigned v, unsigned s) { return __builtin_amdgcn_raw_buffer_load_b64(rs, v, s, 16); }
DEV unsigned ld4(rsrc_t rs, unsigned v, unsigned s) { return __builtin_amdgcn_raw_buffer_load_b32(rs, v, s, 16); }
DEV void st16n(rsrc_t rs, u32x4 d, unsigned v, unsigned s) { __builtin_amdgcn_raw_buffer_store_b128(d, rs, v, s, 16); }
DEV void st16(rsrc_t rs, u32x4 d, unsigned v, unsigned s) {
	st16n(rs, d, v, s);
	asm volatile("s_nop 1" :: "v"(d));
}
DEV void st8(rsrc_t rs, u32x2 d, unsigned v, unsigned s) { __builtin_amdgcn_raw_buffer_store_b64(d, rs, v, s, 16); }
DEV void st4(rsrc_t rs, unsigned d, unsigned v, unsigned s) { __builtin_amdgcn_raw_buffer_store_b32(d, rs, v, s, 16); }
DEV u32x2 trread(const u8* lds) {
	return __builtin_bit_cast(u32x2, __builtin_amdgcn_ds_read_tr16_b64_v4i16((LAS s16x4*)lds));
}
DEV u32x4 cat(u32x2 a, u32x2 b) { return u32x4{a[0], a[1], b[0], b[1]}; }
DEV float cosr(float x) { return __builtin_amdgcn_cosf(x); }
DEV float sinr(float x) { return __builtin_amdgcn_sinf(x); }
DEV unsigned mx4(unsigned m, u32x4 v) { return max(max(max(max(m, v[0]), v[1]), v[2]), v[3]); }
DEV bool anyp(unsigned m) { return __builtin_amdgcn_ballot_w64(m == POIS) != 0; }
DEV void swap32(u32x4& a, u32x4& b) {
#pragma unroll
	for (int i = 0; i < 4; ++i) {
		auto r = __builtin_amdgcn_permlane32_swap(a[i], b[i], false, false);
		a[i] = r[0]; b[i] = r[1];
	}
}
DEV void swap16(u32x4& a, u32x4& b) {
#pragma unroll
	for (int i = 0; i < 4; ++i) {
		auto r = __builtin_amdgcn_permlane16_swap(a[i], b[i], false, false);
		a[i] = r[0]; b[i] = r[1];
	}
}
#define WAIT_BAR(N) asm volatile("s_waitcnt vmcnt(" #N ") lgkmcnt(0)\n\ts_barrier" ::: "memory")
#define LBAR() asm volatile("s_waitcnt lgkmcnt(0)\n\ts_barrier" ::: "memory")
#define FENCE() asm volatile("" ::: "memory")
#define PIN(x) asm volatile("" : "+v"(x))
#define PINA(x) asm volatile("" : "+a"(x))
#define WAITV(N) asm volatile("s_waitcnt vmcnt(" #N ")" ::: "memory")
#define WAITL(N) __builtin_amdgcn_s_waitcnt(0xC07F | ((N) << 8))
#define SB() __builtin_amdgcn_sched_barrier(0)
#define UNLIKELY(x) __builtin_expect(!!(x), 0)

struct Flags {
	u32x4 a[2];
};
DEV void fl_load(Flags& f, rsrc_t rs, int lane, unsigned off) {
	f.a[0] = ld16(rs, lane * 16, off);
	f.a[1] = ld16(rs, lane * 16 + 1024, off);
}
DEV bool fl_bad(const Flags& f, u32x2 tag) {
	unsigned d = 0;
#pragma unroll
	for (int k = 0; k < 2; ++k) d |= ((f.a[k][0] ^ tag[0]) | (f.a[k][1] ^ tag[1])) | ((f.a[k][2] ^ tag[0]) | (f.a[k][3] ^ tag[1]));
	return __builtin_amdgcn_ballot_w64(d != 0) != 0;
}
DEV void fl_check(Flags& f, rsrc_t rs, int lane, unsigned off, u32x2 tag) {
	if (UNLIKELY(fl_bad(f, tag))) {
#pragma unroll 1
		for (int it = 0; it < SPIN; ++it) {
			__builtin_amdgcn_s_sleep(4);
			FENCE();
			fl_load(f, rs, lane, off);
			if (!fl_bad(f, tag)) break;
		}
	}
	FENCE();
}
constexpr int NFL = FS == 8 ? 2 : 4;

DEV void basis(int j, float& kq, float& ph) {
	int c = j >> 1;
	int kk = c ? c : ((j & 1) ? 0 : 224);
	kq = (float)kk * (1.0f / 448.0f);
	ph = (c && (j & 1)) ? 0.25f : 0.0f;
}

struct ColK {
	u32x4 C1[2], S1[2], C2[2], S2[2];
	float twc[8], tws[8], tic[8], tis[8];
};
DEV void col_part(ColK& K, int part, int w, int g, int i16) {
	const float f1 = (float)(16 * w + i16);
	const unsigned odd = (i16 & 1) ? 0x80008000u : 0u;
	if (part < 4) {
		const int i = part;
		float a = f1 * (float)(8 * g + 2 * i) * (1.0f / 64.0f), bb = a + f1 * (1.0f / 64.0f);
		unsigned c = pack2(cosr(a), cosr(bb)), s = pack2(sinr(a), sinr(bb));
		unsigned c2 = c ^ odd, s2 = s ^ odd;
		asm volatile("" : "+v"(c), "+v"(s), "+v"(c2), "+v"(s2));
		K.C1[0][i] = c; K.S1[0][i] = s; K.C1[1][i] = c2; K.S1[1][i] = s2;
	} else if (part < 6) {
		const int i = part & 1;
		float m = (float)i16 * (1.0f / 32.0f);
		float a = m * (float)(4 * g + 2 * i), bb = a + m;
		unsigned c = pack2(cosr(a), cosr(bb)), s = pack2(sinr(a), sinr(bb));
		unsigned c2 = c ^ odd, s2 = s ^ odd, c3 = c ^ 0x80000000u, s3 = s ^ 0x80000000u, c4 = c2 ^ 0x80000000u, s4 = s2 ^ 0x80000000u;
		asm volatile("" : "+v"(c), "+v"(s), "+v"(c2), "+v"(s2), "+v"(c3), "+v"(s3), "+v"(c4), "+v"(s4));
		K.C2[0][i] = c; K.S2[0][i] = s; K.C2[0][2 + i] = c2; K.S2[0][2 + i] = s2;
		K.C2[1][i] = c3; K.S2[1][i] = s3; K.C2[1][2 + i] = c4; K.S2[1][2 + i] = s4;
	} else {
		const int i = part - 6;
		float a = f1 * (float)(16 * (i >> 2) + 4 * g + (i & 3)) * (1.0f / 2048.0f);
		K.twc[i] = cosr(a);
		K.tws[i] = sinr(a);
		float bb = (float)(16 * w + 4 * g + (i & 3)) * (float)(16 * (i >> 2) + i16) * (1.0f / 2048.0f);
		K.tic[i] = cosr(bb) * (1.0f / 2048.0f);
		K.tis[i] = sinr(bb) * (1.0f / 2048.0f);
		asm volatile("" : "+v"(K.twc[i]), "+v"(K.tws[i]), "+v"(K.tic[i]), "+v"(K.tis[i]));
	}
}

constexpr float GK = 0.46866869305f;
#define GC_S "0x409d30fc"
struct GS {
	float t0, t1, u0, u1;
	unsigned o[8];
	f32x4 z[4];
};
DEV void mfv0(f32x16& d, u32x4 a, u32x4 b) { asm volatile("v_mfma_f32_32x32x16_bf16 %0, %1, %2, 0" : "=&v"(d) : "v"(a), "a"(b)); }
DEV void mfv(f32x16& d, u32x4 a, u32x4 b) { asm volatile("v_mfma_f32_32x32x16_bf16 %0, %1, %2, %0" : "+v"(d) : "v"(a), "a"(b)); }
template <int T, bool PREV> DEV void gelu_gap(GS& s, f32x4& z, unsigned& o, float c0, float c1, float r, float k) {
	float z0 = z[T < 3 ? 0 : 2], z1 = z[T < 3 ? 1 : 3];
	const float b0 = z0, b1 = z1;
	if constexpr (T == 0) {
		if constexpr (PREV)
			asm volatile("v_fma_f32 %[z0], %[c0], %[r], %[b0]\n\tv_cvt_pk_bf16_f32 %[o], %[u0], %[u1]\n\tv_fma_f32 %[z1], %[c1], %[r], %[b1]\n\tv_min_u32 %[o], -2, %[o]\n\t"
				"v_fmaak_f32 %[t0], %[z0], %[z0], " GC_S "\n\tv_fmaak_f32 %[t1], %[z1], %[z1], " GC_S
				: [z0] "=&v"(z0), [z1] "=&v"(z1), [o] "=&v"(o), [t0] "=&v"(s.t0), [t1] "=&v"(s.t1) : [c0] "v"(c0), [c1] "v"(c1), [r] "v"(r), [b0] "v"(b0), [b1] "v"(b1), [u0] "v"(s.u0), [u1] "v"(s.u1));
		else
			asm volatile("v_fma_f32 %[z0], %[c0], %[r], %[b0]\n\tv_fma_f32 %[z1], %[c1], %[r], %[b1]\n\t"
				"v_fmaak_f32 %[t0], %[z0], %[z0], " GC_S "\n\tv_fmaak_f32 %[t1], %[z1], %[z1], " GC_S
				: [z0] "=&v"(z0), [z1] "=&v"(z1), [t0] "=&v"(s.t0), [t1] "=&v"(s.t1) : [c0] "v"(c0), [c1] "v"(c1), [r] "v"(r), [b0] "v"(b0), [b1] "v"(b1));
		z[0] = z0; z[1] = z1;
	} else if constexpr (T == 1) {
		asm volatile("v_mul_f32 %[t0], %[z0], %[t0]\n\tv_mul_f32 %[t1], %[z1], %[t1]\n\tv_exp_f32_e64 %[t0], -%[t0]\n\tv_exp_f32_e64 %[t1], -%[t1]"
			: [t0] "+v"(s.t0), [t1] "+v"(s.t1) : [z0] "v"(z0), [z1] "v"(z1));
	} else if constexpr (T == 2) {
		asm volatile("v_fma_f32 %[t0], %[t0], %[k], %[k]\n\tv_fma_f32 %[t1], %[t1], %[k], %[k]\n\tv_rcp_f32 %[t0], %[t0]\n\tv_rcp_f32 %[t1], %[t1]"
			: [t0] "+v"(s.t0), [t1] "+v"(s.t1) : [k] "s"(k));
	} else if constexpr (T == 3) {
		const float y0 = z[0], y1 = z[1];
		asm volatile("v_mul_f32 %[t0], %[y0], %[t0]\n\tv_mul_f32 %[t1], %[y1], %[t1]\n\tv_fma_f32 %[z0], %[c0], %[r], %[b0]\n\t"
			"v_cvt_pk_bf16_f32 %[o], %[t0], %[t1]\n\tv_fma_f32 %[z1], %[c1], %[r], %[b1]\n\tv_min_u32 %[o], -2, %[o]"
			: [z0] "=&v"(z0), [z1] "=&v"(z1), [o] "=&v"(o), [t0] "+v"(s.t0), [t1] "+v"(s.t1) : [c0] "v"(c0), [c1] "v"(c1), [r] "v"(r), [b0] "v"(b0), [b1] "v"(b1), [y0] "v"(y0), [y1] "v"(y1));
		z[2] = z0; z[3] = z1;
	} else if constexpr (T == 4) {
		asm volatile("v_fmaak_f32 %[u0], %[z0], %[z0], " GC_S "\n\tv_fmaak_f32 %[u1], %[z1], %[z1], " GC_S "\n\t"
			"v_mul_f32 %[u0], %[z0], %[u0]\n\tv_mul_f32 %[u1], %[z1], %[u1]\n\tv_exp_f32_e64 %[u0], -%[u0]"
			: [u0] "=&v"(s.u0), [u1] "=&v"(s.u1) : [z0] "v"(z0), [z1] "v"(z1));
	} else if constexpr (T == 5) {
		asm volatile("v_exp_f32_e64 %[u1], -%[u1]\n\tv_fma_f32 %[u0], %[u0], %[k], %[k]\n\tv_fma_f32 %[u1], %[u1], %[k], %[k]\n\tv_rcp_f32 %[u0], %[u0]"
			: [u0] "+v"(s.u0), [u1] "+v"(s.u1) : [k] "s"(k));
	} else {
		asm volatile("v_rcp_f32 %[u1], %[u1]\n\tv_mul_f32 %[u0], %[z0], %[u0]\n\tv_mul_f32 %[u1], %[z1], %[u1]"
			: [u0] "+v"(s.u0), [u1] "+v"(s.u1) : [z0] "v"(z0), [z1] "v"(z1));
	}
}
DEV unsigned gelu2(f32x2 c, f32x2 zb, float r) {
	const f32x2 z = __builtin_elementwise_fma(c, f32x2{r, r}, zb);
	f32x2 t = z * __builtin_elementwise_fma(z, z, f32x2{4.91222953926f, 4.91222953926f});
	t = f32x2{__builtin_amdgcn_exp2f(-t[0]), __builtin_amdgcn_exp2f(-t[1])};
	t = __builtin_elementwise_fma(t, f32x2{GK, GK}, f32x2{GK, GK});
	t = z * f32x2{__builtin_amdgcn_rcpf(t[0]), __builtin_amdgcn_rcpf(t[1])};
	return pack2c(t[0], t[1]);
}
template <int CTRL> DEV float dpp(float x) {
	return __builtin_bit_cast(float, __builtin_amdgcn_update_dpp(0, __builtin_bit_cast(int, x), CTRL, 0xf, 0xf, true));
}

#define CODEPF 62000
#define OUTAUX 16
#define CL0 24
#define HPRE 0
#define HS0 16
#define XD 18
#define SW 13
#define XD0 0
#define CW 16
#define CE 14
#define HKE 1
#define K0 10
#define NF 18
#define GM0 8

template <int I, int N, class F> DEV void sfor(F&& f) {
	if constexpr (I < N) {
		f(IC<I>{});
		sfor<I + 1, N>(f);
	}
}
template <int HI, class F> DEV void p0_gemm(const u8* sm, const u32x4 (&qf)[14], f32x4 (&acc)[4], int g, int i16, F hook) {
	unsigned sg = 2 * (i16 >> 2) + ((i16 >> 1) & 1);
	const u8* base = sm + (8 * (i16 >> 2) + (i16 & 3)) * 896;
	const u8* bp[4] = {base + ((g ^ sg) * 16), base + (((4 + g) ^ sg) * 16), base + ((g ^ sg) * 16) + HI, base + (((4 + g) ^ sg) * 16) + HI};
	u32x4 a[8];
	sfor<-8, 56>([&](auto I) __attribute__((always_inline)) {
		constexpr int i = decltype(I)::value;
		if constexpr (i >= 0) {
			acc[i & 3] = mfma16(a[i & 7], qf[i >> 2], i < 4 ? f32x4{0.f, 0.f, 0.f, 0.f} : acc[i & 3]);
			PINA(acc[i & 3]);
		}
		constexpr int n = i + 8, ks = n >> 2, t = n & 3;
		if constexpr (n < 56) a[n & 7] = *(const u32x4*)(bp[(ks & 1) + (t & 2)] + (ks >> 1) * 128 + (t & 1) * 3584);
		hook(I);
		SB();
	});
}
DEV float dot2(unsigned a, unsigned b, float c) {
	return __builtin_amdgcn_fdot2_f32_bf16(__builtin_bit_cast(bf16x2, a), __builtin_bit_cast(bf16x2, b), c, false);
}
__global__ __launch_bounds__(256, 1) void k_all(const u8* __restrict__ x, const u8* __restrict__ wr, const u8* __restrict__ wi,
		const u8* __restrict__ w1, const u8* __restrict__ b1, const u8* __restrict__ w2, const u8* __restrict__ b2,
		u8* out, u8* ws) {
	__shared__ __attribute__((aligned(16))) u8 sm[163840];
	SB();
	const int tid = threadIdx.x, lane = tid & 63;
	int w = __builtin_amdgcn_readfirstlane(tid >> 6);
	asm volatile("" : "+s"(w));
	const int g = lane >> 4, i16 = lane & 15, h = lane >> 5;
	const int q4 = (lane >> 2) & 3, pc = lane & 3;
	const int b = blockIdx.x;
	{
		unsigned long long pcv;
		asm volatile("s_getpc_b64 %0" : "=s"(pcv));
		dma16(mkrs((const void*)(pcv & ~255ull), CODEPF), sm + L_TOP + 29696 + w * 1024, lane * 16, (((b >> 3) & 15) * 4 + w) * 1024);
		SB();
	}
	const rsrc_t rs = mkrs(ws, WS_END);
	const u32x4 pois = {POIS, POIS, POIS, POIS};
	const u32x2 ep = ld8(rs, 0, O_EPOCH);
	u32x2 tag;
	if (b >= 224) {
		const int sb = b - 224;
		const int li = lane & 31;
		const unsigned v_wt = (lane >> 3) * WTS + (lane & 7) * 16, o_wt = O_WT + (w >> 1) * 8 * WTS + (2 * sb + (w & 1)) * 128;
		{
			const rsrc_t rw = mkrs((w >> 1) ? wi : wr, 2048u * 450u);
#pragma unroll
			for (int n = 0; n < 16; ++n) dma16(rw, sm + (16 * w + n) * 1040, lane * 16, ((900 * sb) & ~15) + (16 * (w & 1) + n) * 28800);
		}
		SB();
#pragma unroll
		for (int n = 0; n < HPRE; ++n) st16n(rs, pois, tid * 16, O_H1 + (sb * 56 + n) * 4096);
		SB();
		const unsigned long long tg = (((unsigned long long)ep[1] << 32) | ep[0]) + 0x9e3779b97f4a7c15ull;
		tag = u32x2{(unsigned)tg, (unsigned)(tg >> 32)};
		asm volatile("s_waitcnt vmcnt(%0)\n\ts_barrier" :: "n"(HPRE) : "memory");
#pragma unroll
		for (int n = 0; n < 14; ++n) {
			const int c = 8 * ((w >> 1) + 2 * n) + (lane >> 3);
			const u8* p = sm + 4 * (lane & 7) * 1040 + ((900 * sb) & 15) + (w & 1) * 450 + (c ? c : 224) * 2;
			u32x4 o;
#pragma unroll
			for (int q = 0; q < 4; ++q) o[q] = (unsigned)*(const u16*)(p + q * 1040) | ((unsigned)*(const u16*)(p + (32 + q) * 1040) << 16);
			st16(rs, o, v_wt, o_wt + n * 16 * WTS);
		}
		WAIT_BAR(0);
		st8(rs, tag, tid ? OOB : 0u, O_FLAGA + b * FS);
#pragma unroll 2
		for (int n = HPRE; n < 56; ++n) st16n(rs, pois, tid * 16, O_H1 + (sb * 56 + n) * 4096);
#pragma unroll
		for (int n = 0; n < 14; ++n) st16n(rs, pois, tid * 16, O_XT2 + (2 * sb + n / 7) * STRIP + (n % 7) * 4096);
		WAIT_BAR(0);
		st8(rs, tag, tid ? OOB : 0u, O_FLAGC + b * FS);
#pragma unroll
		for (int n = 0; n < 14; ++n) st16n(rs, pois, tid * 16, O_XT3 + (2 * sb + n / 7) * STRIP + (n % 7) * 4096);
		asm volatile("s_nop 1" :: "v"(pois));
#pragma unroll 1
		for (int fr = sb * 4 + w; fr < 392; fr += 128) {
			int dt = fr / 28, ks = fr % 28;
			float d = (float)(32 * dt + li);
			float c[8];
#pragma unroll
			for (int i = 0; i < 8; ++i) {
				int j = 16 * ks + 8 * h + i;
				float kq, ph;
				basis(j, kq, ph);
				c[i] = cosr(__builtin_fmaf(kq, d, ph)) * ((j < 2) ? SC1 : SC2);
			}
			st16(rs, u32x4{pack2(c[0], c[1]), pack2(c[2], c[3]), pack2(c[4], c[5]), pack2(c[6], c[7])}, lane * 16, O_QT + fr * 1024);
		}
#pragma unroll 2
		for (int n = 0; n < 56; ++n) st16n(rs, pois, tid * 16, O_H2 + (sb * 56 + n) * 4096);
		WAIT_BAR(0);
		st8(rs, tag, tid ? OOB : 0u, O_FLAGD + b * FS);
		return;
	}
	const int xcd = b & 7, slot = b >> 3, s7 = slot / 7, jt = slot % 7;
	const int pp = 4 * (xcd >> 1) + s7, ft = 7 * (xcd & 1) + jt, pt = 2 * pp + (xcd & 1);
	ColK K;
	u32x2 b1r;
	u32x4 wv[2], cx[3];
	u32x4 W[28][2];
	{
		const int p0 = 4 * xcd + s7;
		const bool fold = p0 < 28;
		const int pf = fold ? p0 : p0 - 28;
		u8* bufW = sm + 57344;
		const unsigned xo = (16 * w + ((lane & 31) >> 2)) * STRIP + h * 64 + pc * 16;
		const unsigned o_xt0 = O_XT0 + 2 * p0 * STRIP + (64 * jt + 16 * w) * 64, v_xt0 = i16 * 64 + g * 16;
		const unsigned o_ss0 = O_SS0 + (64 * p0 + 16 * jt + 4 * w) * 2, v_ss0 = (lane == 0 && jt < 4) ? 0u : OOB;
		const unsigned w1f_o = O_W1F + (2 * pf * 28 + 4 * jt + w) * 1024, v_f = fold ? lane * 16 : OOB;
		const int kk = 4 * pf + g;
		const unsigned kv = ((28 * ((kk >> 1) & 3) + 2 * (kk >> 3) + (kk & 1)) * 1024 + (16 * (w & 1) + i16) * 16) | (fold ? 0u : OOB);
		const unsigned w2f_o = O_W2F + (2 * jt + (w >> 1)) * 112 * 1024;
		const unsigned v_b2 = (g == 0 && p0 == 28) ? i16 * 4 : OOB, o_b2 = O_B2 + (64 * jt + 16 * w) * 4;
		const rsrc_t rx = mkrs(x, 2048u * 896u), r1 = mkrs(w1, 448u * 3584u), r2 = mkrs(w2, 1792u * 896u);
		const int vw[2] = {(lane >> 3) * 3584 + (((lane & 7) ^ (((lane >> 4) & 1) << 1)) * 16), (lane >> 3) * 3584 + (((lane & 7) ^ ((((lane >> 4) & 1) | 2) << 1)) * 16)};
		unsigned vx[7], v2[7];
		u32x4 w2r[7];
		const int j = 64 * jt + 16 * w + i16;
		float kq, ph;
		basis(j, kq, ph);
		const float a0 = (float)(8 * g) * kq + ph;
		const unsigned kodd = (j & 2) ? 0x80008000u : 0u;
		u32x4 qf[14];
#pragma unroll
		for (int v = 0; v < 36; ++v) {
			if (v < 14) {
				const int n = v % 7;
				if (v < 7) {
					const int up = (8 * n + lane >= 56), dr = n + up;
					const unsigned c = ((unsigned)(8 * n + lane - 56 * up) ^ (unsigned)(2 * w + ((dr >> 1) & 1))) * 16;
					vx[n] = dr * 28672 + c;
					v2[n] = dr * 896 + c;
				}
				const int r = 32 * (v / 7) + 8 * w;
				dma16(rx, sm + r * 896 + n * 1024, vx[n], p0 * 896u + r * 28672u);
			} else if (v < 22) {
				if (v < 16) st16n(rs, pois, v_xt0, o_xt0 + (v - 14) * STRIP);
				else if (v == 16) st8(rs, u32x2{POIS, POIS}, v_ss0, o_ss0);
				else if (v < 19) st16n(rs, pois, v_f, w1f_o + (v - 17) * 28 * 1024);
				else if (v < 21) st16n(rs, pois, kv + (v - 19) * 512, w2f_o);
				else st4(rs, POIS, v_b2, o_b2);
			} else {
				const int i = 14 * w + v - 22;
				dma16(r1, bufW + i * 1024, vw[v & 1], i * (8 * 3584) + pf * 128);
			}
			SB();
			if (v >= 8 && v < 22) {
#pragma unroll
				for (int i = 2 * (v & 1); i < 2 * (v & 1) + 2; ++i) {
					const int ks = (v - 8) >> 1;
					unsigned c = pack2(cosr(__builtin_fmaf(kq, (float)(32 * ks + 2 * i), a0)), cosr(__builtin_fmaf(kq, (float)(32 * ks + 2 * i + 1), a0)));
					unsigned c2 = c ^ kodd;
					asm volatile("" : "+v"(c), "+v"(c2));
					qf[ks][i] = c;
					qf[ks + 7][i] = c2;
				}
			} else if (v >= 22) {
				col_part(K, v - 22, w, g, i16);
			}
			SB();
		}
		{
			u32x2 e2 = ep;
			PIN(e2);
			const unsigned long long tg = (((unsigned long long)e2[1] << 32) | e2[0]) + 0x9e3779b97f4a7c15ull;
			tag = u32x2{(unsigned)tg, (unsigned)(tg >> 32)};
		}
		const float scj = (j < 2) ? SC1 : SC2;
		WAIT_BAR(14);
		st8(rs, tag, tid ? OOB : 0u, O_FLAGA + b * FS);
		f32x4 acc[4];
		p0_gemm<28672>(sm, qf, acc, g, i16, [&](auto I) __attribute__((always_inline)) {
			constexpr int i = decltype(I)::value;
			if constexpr (i >= 0 && i % 3 == 0 && i < 48) {
				constexpr int v = i / 3;
				if constexpr (v < 7) dma16(r2, sm + L_TOP + 8 * w * 896 + v * 1024, v2[v], (64 * pf + 16 * w) * 896u);
				else if constexpr (v == 7) dma16(mkrs(b2, 896u), sm + L_TOP + 28672, lane * 16, 0);
				else if constexpr (v < 15) w2r[v - 8] = __builtin_amdgcn_raw_buffer_load_b128(r2, lane * 16, (64 * pf + 16 * w + 8) * 896u + (v - 8) * 1024, 0);
				else b1r = *(const u32x2*)(b1 + (128 * ft + 4 * (lane & 31)) * 2);
			}
		});
#pragma unroll
		for (int u = 0; u < 2; ++u) {
			u32x4 o = {pack2c(acc[2 * u][0] * scj, acc[2 * u][1] * scj), pack2c(acc[2 * u][2] * scj, acc[2 * u][3] * scj),
					   pack2c(acc[2 * u + 1][0] * scj, acc[2 * u + 1][1] * scj), pack2c(acc[2 * u + 1][2] * scj, acc[2 * u + 1][3] * scj)};
			st16(rs, o, v_xt0, o_xt0 + u * STRIP);
		}
		SB();
		{
			const u8* rb = sm + (16 * (jt & 3) + 4 * w + g) * 896 + i16 * 16;
			float s1 = 0.f, s2 = 0.f;
#pragma unroll
			for (int q = 0; q < 4; ++q) {
				u32x4 v = *(const u32x4*)(rb + q * 256);
#pragma unroll
				for (int i = 0; i < 4; ++i) {
					unsigned e = v[i];
					if (q == 3) e = (i16 < 8) ? e : 0u;
					s1 = dot2(e, 0x3f803f80u, s1);
					s2 = dot2(e, e, s2);
				}
			}
			s1 += dpp<0x128>(s1); s2 += dpp<0x128>(s2);
			s1 += dpp<0x124>(s1); s2 += dpp<0x124>(s2);
			s1 += dpp<0x122>(s1); s2 += dpp<0x122>(s2);
			s1 += dpp<0x121>(s1); s2 += dpp<0x121>(s2);
			int sv = __builtin_bit_cast(int, fmaxf(s2 - s1 * s1 * (1.0f / 448.0f), 0.f));
			float t0 = __builtin_bit_cast(float, __builtin_amdgcn_readlane(sv, 0)), t1 = __builtin_bit_cast(float, __builtin_amdgcn_readlane(sv, 16));
			float t2 = __builtin_bit_cast(float, __builtin_amdgcn_readlane(sv, 32)), t3 = __builtin_bit_cast(float, __builtin_amdgcn_readlane(sv, 48));
			st8(rs, u32x2{pack2c(t0, t1), pack2c(t2, t3)}, v_ss0, o_ss0);
		}
		WAIT_BAR(20);
		{
			const unsigned kap = ((q4 >> 1) & 1) | ((g & 1) << 1);
			const u8* rb = bufW + (8 * g + q4) * 128 + (pc & 1) * 8;
			const u8* bp[4];
#pragma unroll
			for (int t = 0; t < 4; ++t) bp[t] = rb + (((2 * t + (pc >> 1)) ^ (kap << 1)) * 16);
			u32x2 lo[8], hi[8];
#pragma unroll
			for (int i = -8; i < 56; ++i) {
				if (i >= 0) {
					acc[i & 3] = mfma16(qf[i >> 2], cat(lo[i & 7], hi[i & 7]), i < 4 ? f32x4{0.f, 0.f, 0.f, 0.f} : acc[i & 3]);
					PINA(acc[i & 3]);
				}
				const int n = i + 8;
				if (n < 56) {
					lo[n & 7] = trread(bp[n & 3] + (n >> 2) * 4096);
					hi[n & 7] = trread(bp[n & 3] + (n >> 2) * 4096 + 512);
				}
				if (i == 0 || i == 4) st16n(rs, pois, xo, O_XT1 + b * 128 + (i >> 2) * 8 * STRIP);
				if (i == 8) st16n(rs, pois, tid < 8 ? tid * 16 : OOB, O_SS1 + b * 128);
				if (i >= 48 && i < 55) *(u32x4*)(sm + 8 * w * 896 + v2[i - 48]) = w2r[i - 48];
				SB();
			}
			const int j0 = 64 * jt + 16 * w + 4 * g;
			const float s0 = (j0 == 0) ? SC1 : SC2, s1 = (j0 == 0) ? 0.f : SC2;
#pragma unroll
			for (int u = 0; u < 2; ++u) {
				unsigned x0 = pack2c(acc[2 * u][0] * s0, acc[2 * u][1] * s1), x1 = pack2c(acc[2 * u][2] * SC2, acc[2 * u][3] * SC2);
				unsigned y0 = pack2c(acc[2 * u + 1][0] * s0, acc[2 * u + 1][1] * s1), y1 = pack2c(acc[2 * u + 1][2] * SC2, acc[2 * u + 1][3] * SC2);
				auto r0 = __builtin_amdgcn_permlane16_swap(x0, y0, false, false);
				auto r1v = __builtin_amdgcn_permlane16_swap(x1, y1, false, false);
				st16(rs, u32x4{r0[0], r1v[0], r0[1], r1v[1]}, v_f, w1f_o + u * 28 * 1024);
			}
		}
		Flags fa;
		fl_load(fa, rs, lane, O_FLAGA);
		LBAR();
		{
			p0_gemm<-(int)L_TOP>(sm + L_TOP, qf, acc, g, i16, [&](auto I) __attribute__((always_inline)) {
				constexpr int i = decltype(I)::value;
				if constexpr (i == CL0) {
					fl_check(fa, rs, lane, O_FLAGA, tag);
					st8(rs, tag, (b | tid) ? OOB : 0u, O_EPOCH);
				}
				if constexpr (i == CL0 || i == CL0 + 4) cx[(i - CL0) >> 2] = ld16(rs, xo, O_XT0 + b * 128 + ((i - CL0) >> 2) * 8 * STRIP);
				if constexpr (i == CL0 + 8) cx[2] = ld16(rs, tid * 16, O_SS0);
				if constexpr (i == CL0 + 12 || i == CL0 + 16) wv[(i - CL0 - 12) >> 2] = ld16(rs, (16 * w + i16) * 128 + g * 16 + (i - CL0 - 12) * 16, O_WT + b * WTS);
			});
#pragma unroll
			for (int hh = 0; hh < 2; ++hh) {
				u32x4 o = {pack2c(acc[hh][0] * scj, acc[hh][1] * scj), pack2c(acc[hh][2] * scj, acc[hh][3] * scj),
						   pack2c(acc[2 + hh][0] * scj, acc[2 + hh][1] * scj), pack2c(acc[2 + hh][2] * scj, acc[2 + hh][3] * scj)};
				st16(rs, o, kv + hh * 512, w2f_o);
			}
			const int kw = 4 * pf + w;
			const unsigned ko = (28 * ((kw >> 1) & 3) + 2 * (kw >> 3) + (kw & 1)) * 1024;
			const unsigned sg = 2 * w + ((q4 >> 1) & 1);
#pragma unroll
			for (int u = 0; u < 2; ++u) {
				unsigned ch = 8 * jt + ((4 * u + 2 * (g & 1) + (pc >> 1)) ^ sg);
				const u8* p = sm + (8 * w + 4 * h + q4) * 896 + ch * 16 + (pc & 1) * 8;
				st16(rs, cat(trread(p + L_TOP), trread(p)), v_f, O_W2O + (2 * jt + u) * 112 * 1024 + ko);
			}
			float c0 = 0.f;
			if (p0 == 28) {
				f32x4 c = {0.f, 0.f, 0.f, 0.f};
#pragma unroll
				for (int ks = 0; ks < 14; ++ks) c = mfma16(*(const u32x4*)(sm + L_TOP + 28672 + 64 * ks + 16 * g), qf[ks], c);
				c0 = c[0];
			}
			st4(rs, min(__builtin_bit_cast(unsigned, c0 * scj), 0xfffffffeu), v_b2, o_b2);
		}
	}
	auto layer = [&](auto LC) __attribute__((always_inline)) {
		constexpr int l = decltype(LC)::value;
		int tid = threadIdx.x;
		PIN(tid);
		const int lane = tid & 63, g = lane >> 4, i16 = lane & 15, h = lane >> 5, q4 = (lane >> 2) & 3, pc = lane & 3;
		const unsigned o_ci = l ? O_XT2 : O_XT0, o_co = l ? O_XT3 : O_XT1, o_h = l ? O_H2 : O_H1;
		const unsigned o_w[2] = {(l ? O_W2O : O_W2F) + ((2 * jt + (w & 1)) * 112 + 7 * w) * 1024, (l ? O_W2O : O_W2F) + ((2 * jt + ((w & 1) ^ 1)) * 112 + 7 * w) * 1024};
		const unsigned o_hl[2] = {o_h + ((2 * pt + (w >> 1)) * 112 + 7 * w) * 1024, o_h + ((2 * pt + ((w >> 1) ^ 1)) * 112 + 7 * w) * 1024};
		const unsigned l16 = lane * 16;
		u32x4 H[28][2];
#define LDF(arr, base, q) arr[(q) >> 1][(q) & 1] = ld16(rs, l16 + ((((q) >> 1) % 7) & 3) * 1024, base[(q) & 1] + ((((q) >> 1) / 7) * 28 + ((((q) >> 1) % 7) & 4)) * 1024)
#define LDW(q) LDF(W, o_w, q)
#define LDH(q) LDF(H, o_hl, q)
#define LDW1(n) do { \
			u8* d_ = sm + (w * 28 + ((n) & ~3)) * 1024; \
			const unsigned s_ = O_W1F + ((4 * ft + w) * 28 + ((n) & ~3)) * 1024; \
			if (((n) & 3) == 0) dma16c<0>(rs, d_, l16, s_); \
			else if (((n) & 3) == 1) dma16c<1024>(rs, d_, l16, s_); \
			else if (((n) & 3) == 2) dma16c<2048>(rs, d_, l16, s_); \
			else dma16c<3072>(rs, d_, l16, s_); } while (0)
		const unsigned o_hs = o_h + ((4 * pp + w) * 112 + 2 * ft) * 1024;
#define HF(k) ((k) < K0 ? 0 : ((k) - K0) * NF / (28 - K0))
#define HK(k) do { SB(); if (l == 0) { _Pragma("unroll") for (int n_ = HF(k); n_ < HF((k) + 1); ++n_) LDW1(n_); } else { LDW(CW + (k)); } SB(); } while (0)
		{
			const unsigned xo = (16 * w + ((lane & 31) >> 2)) * STRIP + h * 64 + pc * 16;
			u32x4 xr, xi, ssp[7];
			unsigned m;
#define COL_LOAD() do { \
				xr = ld16(rs, xo, o_ci + b * 128); xi = ld16(rs, xo, o_ci + b * 128 + 8 * STRIP); \
				if (l == 0) { \
					ssp[0] = ld16(rs, tid * 16, O_SS0); \
				} else { \
					_Pragma("unroll") for (int k = 0; k < 7; ++k) ssp[k] = ld16(rs, tid * 16, O_SS1 + k * 4096); \
				} } while (0)
#define COL_MAX() do { \
				m = mx4(mx4(0, xr), xi); \
				if (l == 0) m = mx4(m, ssp[0]); \
				else { _Pragma("unroll") for (int k = 0; k < 7; ++k) m = mx4(m, ssp[k]); } } while (0)
			if (l == 0) {
				xr = cx[0]; xi = cx[1]; ssp[0] = cx[2];
				LBAR();
			} else {
				COL_LOAD();
			}
			COL_MAX();
			if (UNLIKELY(anyp(m))) {
#pragma unroll 1
				for (int it = 0; it < SPIN; ++it) {
					__builtin_amdgcn_s_sleep(2);
					FENCE();
					if (m == POIS) { COL_LOAD(); COL_MAX(); }
					if (!anyp(m)) break;
				}
			}
			swap32(xr, xi);
			float fr[8], fi[8];
			u8* cs = sm + L_TOP;
			{
				float rr[8];
#pragma unroll
				for (int i = 0; i < 4; ++i) {
					float lo = bflo(ssp[0][i]), hi = bfhi(ssp[0][i]);
					if (l == 1) {
#pragma unroll
						for (int k = 1; k < 7; ++k) { lo = dot2(ssp[k][i], 0x00003f80u, lo); hi = dot2(ssp[k][i], 0x3f800000u, hi); }
					}
					rr[2 * i] = __builtin_amdgcn_rsqf(__builtin_fmaf(lo, 1.0f / 448.0f, EPS));
					rr[2 * i + 1] = __builtin_amdgcn_rsqf(__builtin_fmaf(hi, 1.0f / 448.0f, EPS));
					fr[2 * i] = bflo(xr[i]); fr[2 * i + 1] = bfhi(xr[i]);
					fi[2 * i] = bflo(xi[i]); fi[2 * i + 1] = bfhi(xi[i]);
				}
				HK(0);
				const unsigned imask = b ? 0xffffffffu : 0u;
				u8* p = cs + (tid >> 3) * 160 + (tid & 7) * 16;
				*(u32x4*)p = u32x4{pack2(fr[0] * rr[0], fr[1] * rr[1]), pack2(fr[2] * rr[2], fr[3] * rr[3]), pack2(fr[4] * rr[4], fr[5] * rr[5]), pack2(fr[6] * rr[6], fr[7] * rr[7])};
				HK(1);
				*(u32x4*)(p + 5120) = u32x4{pack2(fi[0] * rr[0], fi[1] * rr[1]) & imask, pack2(fi[2] * rr[2], fi[3] * rr[3]) & imask,
											pack2(fi[4] * rr[4], fi[5] * rr[5]) & imask, pack2(fi[6] * rr[6], fi[7] * rr[7]) & imask};
			}
			HK(2);
			WAIT_BAR(8);
			const f32x4 z4 = {0.f, 0.f, 0.f, 0.f};
			const u32x4 sgn = {0x80008000u, 0x80008000u, 0x80008000u, 0x80008000u};
			f32x4 are[2], aim[2];
			u32x4 Bre, Bim;
			u32x4 mr[4], mi[4];
#pragma unroll
			for (int q = 0; q < 4; ++q) {
				const int qq = (q >> 1) | ((q & 1) << 1);
				const u8* p = cs + (16 * (qq >> 1) + i16) * 160 + 64 * (qq & 1) + 16 * g;
				mr[qq] = *(const u32x4*)p; mi[qq] = *(const u32x4*)(p + 5120);
			}
			SB();
			const u32x4 N1[2] = {K.S1[0] ^ sgn, K.S1[1] ^ sgn}, N2v[2] = {K.S2[0] ^ sgn, K.S2[1] ^ sgn};
			HK(3);
#define PIN4() do { PINA(are[0]); PINA(are[1]); PINA(aim[0]); PINA(aim[1]); } while (0)
#pragma unroll
			for (int ks = 0; ks < 2; ++ks) {
#pragma unroll
				for (int t = 0; t < 2; ++t) {
					are[t] = mfma16(mr[2 * t + ks], K.C1[ks], ks ? are[t] : z4);
					aim[t] = mfma16(mi[2 * t + ks], K.C1[ks], ks ? aim[t] : z4);
				}
				PIN4();
				HK(4 + 2 * ks);
#pragma unroll
				for (int t = 0; t < 2; ++t) {
					are[t] = mfma16(mi[2 * t + ks], K.S1[ks], are[t]);
					aim[t] = mfma16(mr[2 * t + ks], N1[ks], aim[t]);
				}
				PIN4();
				HK(5 + 2 * ks);
			}
#pragma unroll
			for (int t = 0; t < 2; ++t) {
#pragma unroll
				for (int e = 0; e < 4; e += 2) {
					int i = 4 * t + e;
					Bre[i >> 1] = pack2(are[t][e] * K.twc[i] + aim[t][e] * K.tws[i], are[t][e + 1] * K.twc[i + 1] + aim[t][e + 1] * K.tws[i + 1]);
					Bim[i >> 1] = pack2(aim[t][e] * K.twc[i] - are[t][e] * K.tws[i], aim[t][e + 1] * K.twc[i + 1] - are[t][e + 1] * K.tws[i + 1]);
				}
				asm volatile("" : "+v"(Bre[2 * t]), "+v"(Bre[2 * t + 1]), "+v"(Bim[2 * t]), "+v"(Bim[2 * t + 1]));
				HK(8 + t);
			}
#pragma unroll
			for (int t = 0; t < 2; ++t) {
				are[t] = mfma16(K.C2[t], Bre, z4);
				aim[t] = mfma16(K.C2[t], Bim, z4);
			}
			PIN4();
			HK(10);
#pragma unroll
			for (int t = 0; t < 2; ++t) {
				are[t] = mfma16(K.S2[t], Bim, are[t]);
				aim[t] = mfma16(N2v[t], Bre, aim[t]);
			}
			PIN4();
			HK(11);
#pragma unroll
			for (int t = 0; t < 2; ++t) {
#pragma unroll
				for (int e = 0; e < 4; e += 2) {
					float a0 = bflo(wv[t][e]), b0 = bfhi(wv[t][e]), a1 = bflo(wv[t][e + 1]), b1v = bfhi(wv[t][e + 1]);
					Bre[2 * t + (e >> 1)] = pack2(are[t][e] * a0 - aim[t][e] * b0, are[t][e + 1] * a1 - aim[t][e + 1] * b1v);
					Bim[2 * t + (e >> 1)] = pack2(are[t][e] * b0 + aim[t][e] * a0, are[t][e + 1] * b1v + aim[t][e + 1] * a1);
				}
				asm volatile("" : "+v"(Bre[2 * t]), "+v"(Bre[2 * t + 1]), "+v"(Bim[2 * t]), "+v"(Bim[2 * t + 1]));
				HK(12 + t);
			}
#pragma unroll
			for (int t = 0; t < 2; ++t) {
				are[t] = mfma16(Bre, K.C2[t], z4);
				aim[t] = mfma16(Bim, K.C2[t], z4);
			}
			PIN4();
			HK(14);
#pragma unroll
			for (int t = 0; t < 2; ++t) {
				are[t] = mfma16(Bim, N2v[t], are[t]);
				aim[t] = mfma16(Bre, K.S2[t], aim[t]);
			}
			PIN4();
			HK(15);
#pragma unroll
			for (int t = 0; t < 2; ++t) {
				float vr[4], vi[4];
#pragma unroll
				for (int e = 0; e < 4; ++e) {
					vr[e] = are[t][e] * K.tic[4 * t + e] - aim[t][e] * K.tis[4 * t + e];
					vi[e] = aim[t][e] * K.tic[4 * t + e] + are[t][e] * K.tis[4 * t + e];
				}
				u8* p = cs + 10240 + (16 * t + i16) * 160 + 32 * w + 8 * g;
				*(u32x2*)p = u32x2{pack2(vr[0], vr[1]), pack2(vr[2], vr[3])};
				*(u32x2*)(p + 5120) = u32x2{pack2(vi[0], vi[1]), pack2(vi[2], vi[3])};
				HK(16 + t);
			}
			if (l == 0) {
				asm volatile("s_waitcnt vmcnt(%0) lgkmcnt(0)\n\ts_barrier" :: "n"(HF(18)) : "memory");
				st8(rs, tag, tid > 1 ? OOB : tid * (O_FLAGD - O_FLAGC), O_FLAGC + b * FS);
			} else {
				LBAR();
			}
#pragma unroll
			for (int q = 0; q < 4; ++q) {
				const int qq = (q >> 1) | ((q & 1) << 1);
				const u8* p = cs + 10240 + (16 * (qq >> 1) + i16) * 160 + 64 * (qq & 1) + 16 * g;
				mr[qq] = *(const u32x4*)p; mi[qq] = *(const u32x4*)(p + 5120);
			}
			HK(18);
#pragma unroll
			for (int ks = 0; ks < 2; ++ks) {
#pragma unroll
				for (int t = 0; t < 2; ++t) {
					are[t] = mfma16(K.C1[ks], mr[2 * t + ks], ks ? are[t] : z4);
					aim[t] = mfma16(K.C1[ks], mi[2 * t + ks], ks ? aim[t] : z4);
				}
				PIN4();
				HK(19 + 2 * ks);
#pragma unroll
				for (int t = 0; t < 2; ++t) {
					are[t] = mfma16(N1[ks], mi[2 * t + ks], are[t]);
					aim[t] = mfma16(K.S1[ks], mr[2 * t + ks], aim[t]);
				}
				PIN4();
				HK(20 + 2 * ks);
			}
#pragma unroll
			for (int t = 0; t < 2; ++t) {
				u8* p = cs + 20480 + (16 * t + i16) * 272 + 64 * w + 16 * g;
				*(f32x4*)p = are[t];
				*(f32x4*)(p + 8704) = aim[t];
			}
			HK(23);
			HK(25);
			LBAR();
			{
				const u8* p = cs + 20480 + (tid >> 3) * 272 + (tid & 7) * 32;
				f32x4 r0 = *(const f32x4*)p, r1 = *(const f32x4*)(p + 16), i0 = *(const f32x4*)(p + 8704), i1 = *(const f32x4*)(p + 8720);
				HK(24);
				HK(26);
				const float im = b ? 1.0f : 0.0f;
				xr = u32x4{pack2c(fr[0] + r0[0], fr[1] + r0[1]), pack2c(fr[2] + r0[2], fr[3] + r0[3]), pack2c(fr[4] + r1[0], fr[5] + r1[1]), pack2c(fr[6] + r1[2], fr[7] + r1[3])};
				xi = u32x4{pack2c(fi[0] + im * i0[0], fi[1] + im * i0[1]), pack2c(fi[2] + im * i0[2], fi[3] + im * i0[3]),
						   pack2c(fi[4] + im * i1[0], fi[5] + im * i1[1]), pack2c(fi[6] + im * i1[2], fi[7] + im * i1[3])};
				swap32(xr, xi);
				HK(27);
				st16(rs, xr, xo, o_co + b * 128);
				st16(rs, xi, xo, o_co + b * 128 + 8 * STRIP);
			}
		}
		if (tid < 32) *(f32x4*)(sm + L_TAB + tid * 16) = f32x4{bflo(b1r[0]) * GK, bfhi(b1r[0]) * GK, bflo(b1r[1]) * GK, bfhi(b1r[1]) * GK};
		Flags fh;
		if (l == 0) {
#pragma unroll
			for (int n = NF; n < 28; ++n) { LDW1(n); SB(); }
#pragma unroll
			for (int q = 0; q < 4; ++q) { LDW(q); SB(); }
			unsigned o_c = w * 28672 + l16;
			PIN(o_c);
			if (NF == 28) WAITV(6); else WAITV(4);
			unsigned m = 0;
			u32x4 t[28];
#pragma unroll
			for (int n = 0; n < 36; ++n) {
				if (n < 28) t[n] = *(const u32x4*)(sm + o_c + n * 1024);
				if (n >= 8) m = mx4(m, t[n - 8]);
				if ((n & 3) == 3) {
					PIN(m);
					if (4 + (n >> 2) < SW) LDW(4 + (n >> 2));
					SB();
				}
			}
			if (UNLIKELY(anyp(m))) {
#pragma unroll 1
				for (int it = 0; it < SPIN; ++it) {
					__builtin_amdgcn_s_sleep(2);
					FENCE();
					m = 0;
#pragma unroll
					for (int n = 0; n < 28; ++n) LDW1(n);
					WAITV(0);
#pragma unroll
					for (int n = 0; n < 28; ++n) m = mx4(m, *(const u32x4*)(sm + o_c + n * 1024));
					if (!anyp(m)) break;
				}
			}
#pragma unroll
			for (int q = 13; q < SW; ++q) { LDW(q); SB(); }
			LBAR();
			if (XD0) __builtin_amdgcn_s_sleep(XD0);
		} else {
			LBAR();
			__builtin_amdgcn_s_sleep(XD);
		}
		const unsigned o_xs = o_co + (4 * pp + w) * STRIP;
		u32x4 xq[14], qb[14];
		unsigned o_q[3] = {L_TOP + w * 8192 + l16, L_TOP + 32768 + w * 3072 + l16, 57344 + w * 3072 + l16};
		const u8* tq = sm + w * 14336 + (8 * h + q4) * 64 + (g & 1) * 32 + (pc & 1) * 16 + (pc >> 1) * 8;
#define LDQ(k) qb[k] = *(const u32x4*)(sm + ((k) < 8 ? o_q[0] + (k) * 1024 : (k) < 11 ? o_q[1] + ((k) - 8) * 1024 : o_q[2] + ((k) - 11) * 1024))
#define LDXQ(k) xq[k] = cat(trread(tq + (k) * 1024), trread(tq + (k) * 1024 + 256))
		{
			u32x4 xs[28], B[28];
#define LDX(n) xs[n] = ld16(rs, l16 + ((n) & 3) * 1024, o_xs + ((n) & ~3) * 1024)
#pragma unroll
			for (int n = 0; n < 12; ++n) { LDX(n); SB(); }
			f32x16 ac[4];
			f32x4 gr[2];
			f32x16 z16;
#pragma unroll
			for (int r = 0; r < 16; ++r) z16[r] = 0.f;
			const f32x4 z4 = {0.f, 0.f, 0.f, 0.f};
			unsigned o_wp = L_TOP + w * 8192 + l16, o_tp = L_TOP + w * 8192 + (8 * h + q4) * 64 + (g & 1) * 32 + (pc & 1) * 16 + (pc >> 1) * 8;
			unsigned o_a1 = l16 + 57344, o_tab = L_TAB + h * 16;
			PIN(o_wp); PIN(o_tp); PIN(o_a1); PIN(o_tab);
			u8* wp = sm + o_wp;
			const u8* tp = sm + o_tp;
			const u8* ap[2] = {sm + l16, sm + o_a1};
#define LDA(q) A[(q) % NA] = *(const u32x4*)(ap[(q) / 56] + ((q) % 56) * 1024)
			constexpr int NA = 8, DA = 5;
			u32x4 A[NA];
			u32x2 tlo[4];
			float r2;
			sfor<0, 8>([&](auto C) __attribute__((always_inline)) {
				constexpr int c = decltype(C)::value;
				if constexpr (c < 7) {
					unsigned m = 0;
#pragma unroll
					for (int k = 0; k < 4; ++k) m = mx4(m, xs[4 * c + k]);
					if (UNLIKELY(anyp(m))) {
#pragma unroll 1
						for (int it = 0; it < SPIN; ++it) {
							__builtin_amdgcn_s_sleep(2);
							FENCE();
							m = 0;
#pragma unroll
							for (int k = 0; k < (c ? 4 : 12); ++k) LDX(4 * c + k);
#pragma unroll
							for (int k = 0; k < 4; ++k) m = mx4(m, xs[4 * c + k]);
							if (!anyp(m)) break;
						}
					}
				}
				SB();
#pragma unroll
				for (int i = 0; i < 8; ++i) {
					if constexpr (c > 0) {
						const int k = (c < 7) ? (i >> 1) : (i & 3), ks = 4 * (c - 1) + k;
						if ((c < 7) ? (i & 1) : (i >> 2)) {
							if (ks) mfv(ac[0], A[ks % NA], B[ks]); else mfv0(ac[0], A[0], B[0]);
						} else {
							const u32x2 b0 = {B[ks][0], B[ks][1]}, b1v = {B[ks][2], B[ks][3]};
							if (ks == 0) {
								const u32x2 m0 = {b0[0] & (h ? 0xffffffffu : 0x0000ffffu), b0[1]};
								asm volatile("s_nop 1\n\tv_mfma_f32_4x4x4_16b_bf16 %0, %2, %3, 0\n\tv_mfma_f32_4x4x4_16b_bf16 %1, %4, %4, 0" : "=&v"(gr[0]), "=&v"(gr[1]) : "v"(m0), "a"(b0), "a"(b1v));
							} else if (c < 7) {
								asm volatile("v_mfma_f32_4x4x4_16b_bf16 %0, %2, %2, %0\n\tv_mfma_f32_4x4x4_16b_bf16 %1, %3, %3, %1" : "+v"(gr[0]), "+v"(gr[1]) : "a"(b0), "a"(b1v));
							} else {
								asm volatile("s_nop 0\n\tv_mfma_f32_4x4x4_16b_bf16 %0, %2, %2, %0\n\tv_mfma_f32_4x4x4_16b_bf16 %1, %3, %3, %1" : "+v"(gr[0]), "+v"(gr[1]) : "a"(b0), "a"(b1v));
							}
						}
					}
					if constexpr (c < 7) {
#pragma unroll
						for (int o = 2 * i; o < 2 * i + 2; ++o) {
							if (o < 4) *(u32x4*)(wp + ((4 * c + o) & 7) * 1024) = xs[4 * c + o];
							else if (o < 12) {
								const int k = (o - 4) >> 1, ks = 4 * c + k;
								if (o & 1) B[ks] = cat(tlo[k], trread(tp + (ks & 7) * 1024 + 256));
								else tlo[k] = trread(tp + (ks & 7) * 1024);
							} else {
								LDA(4 * c + o - 12);
							}
						}
						if (i & 1) {
							const int n = 4 * c + 12 + (i >> 1);
							if (n < 28) LDX(n);
							else if (l == 0 || n + CW < 56) LDW(l ? n + CW : n + SW - 28);
						}
					} else {
						if (i >= 4 && i - 4 < DA - 1) LDA(28 + i - 4);
						if (l == 0 && i == 0) fl_load(fh, rs, lane, O_FLAGC);
						if (i == 6) {
							asm volatile("s_nop 1" : "+v"(gr[0]), "+v"(gr[1]));
							f32x4 s4 = gr[0] + gr[1];
							float sa = (lane & 1) ? s4[1] : s4[0], sb = (lane & 1) ? s4[3] : s4[2];
							int si = __builtin_bit_cast(int, (lane & 2) ? sb : sa);
							auto sw = __builtin_amdgcn_permlane32_swap(si, si, false, false);
							const unsigned s0 = sw[0], s1 = sw[1];
							float v = (__builtin_bit_cast(float, s0) + __builtin_bit_cast(float, s1)) * (1.0f / 448.0f);
							float r1 = __builtin_amdgcn_rsqf(v + EPS);
							r2 = (r1 * GK) * __builtin_amdgcn_rsqf(__builtin_fmaf(v * r1, r1, EPS));
							PIN(r2);
						}
					}
					SB();
				}
			});
			const u8* tab = sm + o_tab;
			GS gs;
			float gk = GK;
			asm volatile("" : "+s"(gk));
			f32x4 zl[4];
			gs.z[0] = *(const f32x4*)tab;
			sfor<1, 4>([&](auto ST) __attribute__((always_inline)) {
				constexpr int st = decltype(ST)::value;
				sfor<0, 28>([&](auto I) __attribute__((always_inline)) {
					constexpr int i = decltype(I)::value, q = 28 * st + i;
					constexpr int T = (i + 6) % 7, gq = ((i + 27) % 28) / 7;
					if constexpr (l == 1 && i == 0 && st > 1) asm volatile("s_barrier" ::: "memory");
					if constexpr (i == 0) mfv0(ac[st], A[q % NA], B[0]);
					else mfv(ac[st], A[q % NA], B[i]);
					SB();
					if constexpr (q + DA - 1 < 112) LDA(q + DA - 1);
					else zl[q + DA - 1 - 112] = *(const f32x4*)(tab + 384 + (q + DA - 1 - 112) * 32);
					if constexpr (T == 4 && (st < 3 || gq < 3)) gs.z[(gq + 1) & 3] = *(const f32x4*)(tab + (st - 1) * 128 + (gq + 1) * 32);
					if constexpr (i == 21 || (i == 7 && st > 1)) st16n(rs, i == 21 ? u32x4{gs.o[0], gs.o[1], gs.o[2], gs.o[3]} : u32x4{gs.o[4], gs.o[5], gs.o[6], gs.o[7]}, l16 + (i == 21 ? 0 : 1024), o_hs + (st - (i == 21 ? 1 : 2)) * 28 * 1024);
					if constexpr (l == 0 && (i & 1) == 0 && SW + 12 + 14 * (st - 1) + (i >> 1) < 56) LDW(SW + 12 + 14 * (st - 1) + (i >> 1));
					if constexpr (l == 1 && (i & 1) == 0) {
						constexpr int k = st == 1 ? i / 2 - 1 : st == 2 ? i / 2 : 11 + i / 2;
						if constexpr (st == 2) {
							constexpr int k0 = k - ((k % 7) & 3);
							dma16c<(k - k0) * 1024>(rs, sm + w * 14336 + k0 * 1024, l16, O_XT3 + (2 * pt + ((w >> 1) ^ (k / 7))) * STRIP + (7 * w + k0 % 7) * 1024);
						} else if constexpr (k >= 0 && k < (st == 1 ? 11 : 14)) {
							constexpr int k0 = k < 4 ? 0 : k < 7 ? 4 : k < 8 ? 7 : k < 11 ? 8 : 11;
							dma16c<(k - k0) * 1024>(rs, sm + (k0 < 8 ? L_TOP + w * 8192 + k0 * 1024 : k0 < 11 ? L_TOP + 32768 + w * 3072 : 57344 + w * 3072), l16,
								O_QT + ((2 * jt + ((w & 1) ^ (k / 7))) * 28 + 7 * w + k0 % 7) * 1024);
						}
					}
					if constexpr (st == 3 && i >= HS0 && ((i - HS0) & 1) == 0 && (i - HS0) / 2 < 14) LDH((i - HS0) / 2);
					SB();
					if constexpr (i & 1) WAITL(T == 4 || T == 5 ? 3 : 2);
					SB();
					if constexpr (i == 0 && st == 1) {
						asm volatile("s_nop 7");
					} else {
						constexpr int e = 4 * gq + (T < 3 ? 0 : 2);
						gelu_gap<T, (st > 1 || gq > 0)>(gs, gs.z[gq], gs.o[(T == 0 ? 2 * gq + 7 : 2 * gq) & 7], ac[st - 1][e], ac[st - 1][e + 1], r2, gk);
					}
					if constexpr (l == 0 && st == 1 && i == 14) fl_check(fh, rs, lane, O_FLAGC, tag);
					SB();
				});
			});
			asm volatile("v_rcp_f32 %1, %1\n\tv_mul_f32 %0, %4, %0\n\tv_mul_f32 %1, %5, %1\n\ts_nop 0\n\tv_cvt_pk_bf16_f32 %2, %0, %1\n\ts_nop 0\n\tv_min_u32 %2, -2, %2\n\ts_nop 0"
				: "+v"(gs.u0), "+v"(gs.u1), "=&v"(gs.o[7]), "+v"(ac[3]) : "v"(gs.z[3][2]), "v"(gs.z[3][3]));
			st16(rs, u32x4{gs.o[4], gs.o[5], gs.o[6], gs.o[7]}, l16 + 1024, o_hs + 2 * 28 * 1024);
			SB();
			sfor<0, 4>([&](auto Q) __attribute__((always_inline)) {
				constexpr int q = decltype(Q)::value;
				gs.o[2 * q] = gelu2(f32x2{ac[3][4 * q], ac[3][4 * q + 1]}, f32x2{zl[q][0], zl[q][1]}, r2);
				gs.o[2 * q + 1] = gelu2(f32x2{ac[3][4 * q + 2], ac[3][4 * q + 3]}, f32x2{zl[q][2], zl[q][3]}, r2);
				asm volatile("" : "+v"(gs.o[2 * q]), "+v"(gs.o[2 * q + 1]));
				if constexpr (q & 1) st16(rs, u32x4{gs.o[2 * q - 2], gs.o[2 * q - 1], gs.o[2 * q], gs.o[2 * q + 1]}, l16 + (q >> 1) * 1024, o_hs + 3 * 28 * 1024);
				SB();
				if constexpr (q < 3) {
					LDH((28 - HS0 + 1) / 2 + 2 * q);
					LDH((28 - HS0 + 1) / 2 + 2 * q + 1);
				} else {
#pragma unroll
					for (int k = (28 - HS0 + 1) / 2 + 6; k < 14; ++k) LDH(k);
				}
				SB();
				if constexpr (l == 1 && q == 2) {
					PIN(o_q[0]); PIN(o_q[1]); PIN(o_q[2]);
					WAITV(16);
					LDXQ(0); LDQ(0); LDQ(7); LDXQ(7);
					SB();
				}
			});
		}
		if (l == 0) LBAR();
		{
			f32x16 acc[4], z16;
#pragma unroll
			for (int r = 0; r < 16; ++r) z16[r] = 0.f;
			u32x4 res[2];
			unsigned bias = 0;
			u8* ex = sm + L_TOP;
			unsigned m;
#define H_CHECK(n0, n1) do { \
				m = 0; \
				_Pragma("unroll") for (int k = (n0); k < (n1); ++k) m = mx4(mx4(m, H[k][0]), H[k][1]); \
			} while (0)
#define H_RETRY(n0, n1) \
			if (UNLIKELY(anyp(m))) { \
				_Pragma("unroll 1") for (int it = 0; it < SPIN; ++it) { \
					__builtin_amdgcn_s_sleep(2); \
					FENCE(); \
					_Pragma("unroll") for (int k = 2 * (n0); k < 2 * (n1); ++k) LDH(k); \
					H_CHECK(n0, n1); \
					if (!anyp(m)) break; \
				} \
			}
#define LQ(m) do { if (l == 1 && (m) >= GM0 && (((m) - GM0) & 1) == 0 && 14 + ((m) - GM0) / 2 < 56) LDH(14 + ((m) - GM0) / 2); } while (0)
			if (l == 1) {
#pragma unroll
				for (int k = 0; k < 7; ++k) {
#pragma unroll
					for (int a = 0; a < 4; ++a) {
						acc[a] = mfma32(xq[7 * (a >> 1) + k], qb[7 * (a & 1) + k], k ? acc[a] : z16);
						PINA(acc[a]);
						if (k == 0) {
							if (a & 1) { LDQ(8 + (a >> 1)); LDXQ(8 + (a >> 1)); }
							else { LDXQ(1 + (a >> 1)); LDQ(1 + (a >> 1)); }
						} else if (k < 5) {
							if (a == 0) LDXQ(k + 2);
							if (a == 1) { LDQ(k + 2); LDQ(k + 9); }
							if (a == 2) LDXQ(k + 9);
						}
						LQ(4 * k + a);
						SB();
					}
				}
				asm volatile("s_barrier" ::: "memory");
			}
			H_CHECK(0, 1);
			H_RETRY(0, 1)
			SB();
#pragma unroll
			for (int n = 0; n < 21; ++n) {
				acc[0] = mfma32(H[n][0], W[n][0], (n || l) ? acc[0] : z16);
				PINA(acc[0]);
				if (l == 0) LDH(2 * n + 14);
				LQ(28 + 4 * n);
				SB();
				acc[1] = mfma32(H[n][0], W[n][1], (n || l) ? acc[1] : z16);
				PINA(acc[1]);
				LQ(29 + 4 * n);
				if (n < 20) H_CHECK(n + 1, n + 2);
				SB();
				acc[2] = mfma32(H[n][1], W[n][0], (n || l) ? acc[2] : z16);
				PINA(acc[2]);
				if (l == 0) LDH(2 * n + 15);
				LQ(30 + 4 * n);
				SB();
				acc[3] = mfma32(H[n][1], W[n][1], (n || l) ? acc[3] : z16);
				PINA(acc[3]);
				LQ(31 + 4 * n);
				if (l == 0) {
					if (n == 8) bias = ld4(rs, (lane & 31) * 4, O_B2 + (64 * jt + 32 * (w & 1)) * 4);
					if (n == 9 || n == 10) res[n - 9] = ld16(rs, (lane & 31) * 64 + h * 16 + (n - 9) * 32, O_XT1 + (2 * pt + (w >> 1)) * STRIP + (64 * jt + 32 * (w & 1)) * 64);
				} else {
					if (n == 17) bias = (unsigned)*(const u16*)(b2 + (64 * jt + 32 * (w & 1) + (lane & 31)) * 2) << 16;
				}
				SB();
				if (n < 20) { H_RETRY(n + 1, n + 2) }
			}
			H_CHECK(21, 28);
			H_RETRY(21, 28)
			SB();
#pragma unroll
			for (int a = 1; a < 5; ++a) {
#pragma unroll
				for (int n = 21; n < 28; ++n) {
					acc[a & 3] = mfma32(H[n][(a >> 1) & 1], W[n][a & 1], acc[a & 3]);
					PINA(acc[a & 3]);
					if (a > 1 && n >= 23 && n < 27) {
						const int q = n - 23, i = a - 1;
						*(f32x4*)(ex + (w ^ i) * 12288 + (i - 1) * 4096 + l16 + q * 1024) = f32x4{acc[i][4 * q], acc[i][4 * q + 1], acc[i][4 * q + 2], acc[i][4 * q + 3]};
					}
					SB();
				}
			}
			if (l == 0) {
				const float t1 = acc[1][0], t2 = acc[2][0], ts = t1 + t2;
				if (UNLIKELY(__builtin_amdgcn_ballot_w64(ts != ts || bias == POIS) != 0)) {
#pragma unroll 1
					for (int it = 0; it < SPIN && anyp(bias); ++it) {
						FENCE();
						bias = ld4(rs, (lane & 31) * 4, O_B2 + (64 * jt + 32 * (w & 1)) * 4);
					}
#pragma unroll
					for (int a = 0; a < 4; ++a) acc[a] = z16;
#pragma unroll 1
					for (int n = 0; n < 28; ++n) {
						const unsigned fo = ((n / 7) * 28 + n % 7) * 1024;
						const u32x4 h0 = ld16(rs, l16, o_hl[0] + fo), h1 = ld16(rs, l16, o_hl[1] + fo);
						u32x4 w0, w1v;
#pragma unroll 1
						for (int it = 0; it < SPIN; ++it) {
							FENCE();
							w0 = ld16(rs, l16, o_w[0] + fo);
							w1v = ld16(rs, l16, o_w[1] + fo);
							if (!anyp(mx4(mx4(0, w0), w1v))) break;
						}
						acc[0] = mfma32(h0, w0, acc[0]);
						acc[1] = mfma32(h0, w1v, acc[1]);
						acc[2] = mfma32(h1, w0, acc[2]);
						acc[3] = mfma32(h1, w1v, acc[3]);
					}
#pragma unroll
					for (int i = 1; i < 4; ++i) {
#pragma unroll
						for (int q = 0; q < 4; ++q)
							*(f32x4*)(ex + (w ^ i) * 12288 + (i - 1) * 4096 + l16 + q * 1024) = f32x4{acc[i][4 * q], acc[i][4 * q + 1], acc[i][4 * q + 2], acc[i][4 * q + 3]};
					}
				}
			}
			LBAR();
			float v[16];
			const float bf = __builtin_bit_cast(float, bias);
#pragma unroll
			for (int q = 0; q < 4; ++q) {
				const u8* p = ex + w * 12288 + q * 1024 + l16;
				f32x4 s = (*(const f32x4*)p + *(const f32x4*)(p + 4096)) + *(const f32x4*)(p + 8192);
#pragma unroll
				for (int e = 0; e < 4; ++e) {
					v[4 * q + e] = (acc[0][4 * q + e] + bf) + s[e];
					if (l == 0) v[4 * q + e] += (e & 1) ? bfhi(res[q >> 1][2 * (q & 1) + (e >> 1)]) : bflo(res[q >> 1][2 * (q & 1) + (e >> 1)]);
				}
			}
			u32x4 X = {pack2c(v[0], v[1]), pack2c(v[2], v[3]), pack2c(v[4], v[5]), pack2c(v[6], v[7])};
			u32x4 Y = {pack2c(v[8], v[9]), pack2c(v[10], v[11]), pack2c(v[12], v[13]), pack2c(v[14], v[15])};
			if (l == 0) {
				swap16(X, Y);
				const unsigned o = O_XT2 + (2 * pt + (w >> 1)) * STRIP + (64 * jt + 32 * (w & 1)) * 64;
				const unsigned vo = i16 * 64 + (g & 1) * 32 + h * 16;
				st16(rs, X, vo, o);
				st16(rs, Y, vo + 1024, o);
				SB();
				const float keep = (64 * jt + 32 * (w & 1) + (lane & 31) == 1) ? 0.f : 1.f;
				float s[8];
#pragma unroll
				for (int i = 0; i < 8; ++i) {
					int a = __builtin_bit_cast(int, v[i] * v[i] * keep), c = __builtin_bit_cast(int, v[i + 8] * v[i + 8] * keep);
					auto r = __builtin_amdgcn_permlane16_swap(a, c, false, false);
					const unsigned r0 = r[0], r1 = r[1];
					s[i] = __builtin_bit_cast(float, r0) + __builtin_bit_cast(float, r1);
				}
#pragma unroll
				for (int i = 0; i < 4; ++i) s[i] = ((lane & 8) ? s[i + 4] : s[i]) + dpp<0x128>((lane & 8) ? s[i] : s[i + 4]);
#pragma unroll
				for (int i = 0; i < 2; ++i) s[i] = ((lane & 4) ? s[i + 2] : s[i]) + dpp<0x141>((lane & 4) ? s[i] : s[i + 2]);
				s[0] = ((lane & 2) ? s[1] : s[0]) + dpp<0x4e>((lane & 2) ? s[0] : s[1]);
				s[0] += dpp<0xb1>(s[0]);
				if (!(lane & 1)) *(float*)(ex + w * 12288 + 11264 + (16 * (g & 1) + 8 * h + ((lane >> 1) & 7)) * 4) = s[0];
				LBAR();
				if (tid < 8) {
					const u8* p = ex + (tid >> 2) * 24576 + 11264 + (tid & 3) * 32;
					f32x4 s0 = *(const f32x4*)p + *(const f32x4*)(p + 12288), s1 = *(const f32x4*)(p + 16) + *(const f32x4*)(p + 12304);
					st16(rs, u32x4{pack2c(s0[0], s0[1]), pack2c(s0[2], s0[3]), pack2c(s1[0], s1[1]), pack2c(s1[2], s1[3])}, tid * 16, O_SS1 + jt * 4096 + pt * 128);
				}
				fl_load(fh, rs, lane, O_FLAGD);
				SB();
				int ln = lane;
				PIN(ln);
#pragma unroll
				for (int part = 0; part < 14; ++part) {
					const unsigned o_n[2] = {o_w[0] + FR_B, o_w[1] + FR_B};
#pragma unroll
					for (int q = min(part, CE) * CW / CE; q < min(part + 1, CE) * CW / CE; ++q) LDF(W, o_n, q);
					SB();
					col_part(K, part, w, ln >> 4, ln & 15);
					SB();
				}
				fl_check(fh, rs, lane, O_FLAGD, tag);
			} else {
				u8* im = ex + w * 12288;
				*(u32x4*)(im + (lane & 31) * 64 + h * 16) = X;
				*(u32x4*)(im + (lane & 31) * 64 + h * 16 + 32) = Y;
#pragma unroll
				for (int u = 0; u < 2; ++u) {
					const u8* p = im + (8 * g + q4) * 64 + 32 * u + 8 * pc;
					__builtin_amdgcn_raw_buffer_store_b128(cat(trread(p), trread(p + 256)), mkrs(out, 2048u * 896u), i16 * 28672 + g * 16,
						(32 * (32 * (w >> 1) + 16 * u) + pt) * 896 + (64 * jt + 32 * (w & 1)) * 2, OUTAUX);
				}
			}
		}
	};
	layer(IC<0>{});
	layer(IC<1>{});
}

extern "C" void kernel_launch(void* const* d_in, const int* in_sizes, int n_in, void* d_out, int out_size, void* d_ws, size_t ws_size,
		hipStream_t stream) {
	k_all<<<256, 256, 0, stream>>>((const u8*)d_in[0], (const u8*)d_in[1], (const u8*)d_in[2], (const u8*)d_in[4], (const u8*)d_in[5],
		(const u8*)d_in[6], (const u8*)d_in[7], (u8*)d_out, (u8*)d_ws);
}
```
